```python
import torch

def _softmax(x, axis=-1):
    return torch.softmax(x, dim=axis)
def _gelu(x):
    return torch.nn.functional.gelu(x, approximate="tanh")
def _silu(x):
    return torch.nn.functional.silu(x)
def _sigmoid(x):
    return torch.sigmoid(x)
def _layernorm(x, axis=-1, eps=1e-5):
    m = x.mean(dim=axis, keepdim=True)
    v = x.var(dim=axis, keepdim=True, unbiased=False)
    return (x - m) / torch.sqrt(v + eps)
def _scatter_add(tgt, idx, src, axis=0):
    return tgt.clone().index_add_(axis, idx, src)
def _segment_sum(src, seg, n):
    out = torch.zeros((n,) + src.shape[1:], dtype=src.dtype)
    return out.index_add_(0, seg, src)
def _pad(x, pw):
    flat = tuple(v for pair in reversed(pw) for v in pair)
    return torch.nn.functional.pad(x, flat)

kernel_name = 'sequential_preLN__sliding_block_cached__dense_activation_relu__decode'

def setup_inputs():

    _w = (lambda a: (lambda t: t.to(torch.bfloat16) if t.is_floating_point() else t)(torch.as_tensor(a)))
    def _A_setup():
        B, Sq, D = 8, 1, 1024
        Hq, Hkv, Sk, Dh = 32, 4, 4096, 128
        assert Hq % Hkv == 0, (Hq, Hkv)
        return {
            "x": _w((lambda *s: torch.randn(*s, dtype=torch.bfloat16) * 0.05)(B, Sq, D)),
            "k_cache": _w((lambda *s: torch.randn(*s, dtype=torch.bfloat16) * 3.2)(B, Hkv, Sk, Dh)),
            "v_cache": _w((lambda *s: torch.randn(*s, dtype=torch.bfloat16) * 0.4)(B, Hkv, Sk, Dh)),
            "wq": _w((lambda *s: torch.randn(*s, dtype=torch.bfloat16) * 3.2)(D, Hq * Dh) * (D ** -0.5)),
            "wo": _w((lambda *s: torch.randn(*s, dtype=torch.bfloat16) * 0.4)(Hq * Dh, D) * ((Hq * Dh) ** -0.5)),
        }
    _A_in = _A_setup()

    def _B_setup():
        B, S, D, Dff = 8, 1, 1024, 3584
        return {
            "x": _w((lambda *s: torch.randn(*s, dtype=torch.bfloat16) * 0.05)(B, S, D)),
            "w1": _w((lambda *s: torch.randn(*s, dtype=torch.bfloat16) * 0.05)(D, Dff)),
            "w2": _w((lambda *s: torch.randn(*s, dtype=torch.bfloat16) * 0.05)(Dff, D)),
        }
    _B_in = _B_setup()

    return {
        "A_x": _A_in["x"],
        "A_k_cache": _A_in["k_cache"],
        "A_v_cache": _A_in["v_cache"],
        "A_wq": _A_in["wq"],
        "A_wo": _A_in["wo"],
        "B_x": _B_in["x"],
        "B_w1": _B_in["w1"],
        "B_w2": _B_in["w2"],
    }

def reference(A_x, A_k_cache, A_v_cache, A_wq, A_wo, B_x, B_w1, B_w2):
    def _A_ref(x, k_cache, v_cache, wq, wo):
        B, Sq, D = x.shape
        Hq, Dh = 32, 128
        Hkv, Sk = k_cache.shape[1], k_cache.shape[2]
        W = min(4096, max(256, Sk // 4))
        rep = Hq // Hkv
        q = (lambda x, *d: x.permute(*d))((x @ wq).reshape(B, Sq, Hq, Dh), 0, 2, 1, 3)
        k = (lambda x, n, axis: torch.repeat_interleave(x, n, dim=axis))(k_cache[:, :, Sk - W:, :], rep, axis=1)
        v = (lambda x, n, axis: torch.repeat_interleave(x, n, dim=axis))(v_cache[:, :, Sk - W:, :], rep, axis=1)
        scores = torch.einsum("bhqd,bhkd->bhqk", q, k) * (Dh ** -0.5)
        p = _softmax(scores, axis=-1)
        o = torch.einsum("bhqk,bhkd->bhqd", p, v)
        return (lambda x, *d: x.permute(*d))(o, 0, 2, 1, 3).reshape(B, Sq, Hq * Dh) @ wo

    def _B_ref(x, w1, w2):
        h = torch.einsum("bsd,df->bsf", x, w1)
        zero = torch.zeros_like(h)
        h = torch.maximum(h, zero)
        return torch.einsum("bsf,fd->bsd", h, w2)

    x = A_x
    h = x + _A_ref(_layernorm(x, axis=-1), A_k_cache, A_v_cache, A_wq, A_wo)
    h2 = h + _B_ref(_layernorm(h, axis=-1), B_w1, B_w2)
    return h2
```

```cpp
#include <hip/hip_runtime.h>
#include <hip/hip_bf16.h>
#include <stdint.h>
#define MIMIC 0
typedef unsigned short u16;
typedef unsigned v4u __attribute__((ext_vector_type(4)));
constexpr int B=8, D=1024, HQ=32, HKV=4, SK=4096, DH=128, W=1024, DFF=3584;
__device__ __forceinline__ float bf2f(u16 v){ return __uint_as_float(((unsigned)v)<<16); }
__device__ __forceinline__ u16 f2bf(float f){ unsigned u=__float_as_uint(f); u += 0x7FFFu + ((u>>16)&1u); return (u16)(u>>16); }
__device__ __forceinline__ float rb(float f){
  return f;
}
__device__ void ln_rows(const float* xin  , float* y  , float* red, int tid){
  int w=tid>>6, l=tid&63;
  for(int r=2*w;r<2*w+2;++r){
    float s=0; for(int i=l;i<D;i+=64) s+=xin[r*D+i];
    for(int o=32;o;o>>=1) s+=__shfl_xor(s,o);
    float mean=s/D;
    float v=0; for(int i=l;i<D;i+=64){ float d=xin[r*D+i]-mean; v+=d*d; }
    for(int o=32;o;o>>=1) v+=__shfl_xor(v,o);
    float var=rb(v/D); float m=rb(mean);
    float den=rb(sqrtf(rb(var+1e-5f)));
    for(int i=l;i<D;i+=64) y[r*D+i]=rb(rb(xin[r*D+i]-m)/den);
  }
}
template<int N>
__device__ void gemv32(const float* in, int ldin, int k0, int KS, const u16* Wt, int c0, float* red  , float* out  , int tid){
  int kg=tid>>2, c=tid&3;
  float acc[8][8];
  for(int b=0;b<8;++b) for(int j=0;j<8;++j) acc[b][j]=0;
  for(int k=kg;k<KS;k+=64){
    v4u wv=*(const v4u*)(Wt+(size_t)(k0+k)*N+c0+c*8);
    float wf[8];
    for(int j=0;j<4;++j){ wf[2*j]=__uint_as_float(wv[j]<<16); wf[2*j+1]=__uint_as_float(wv[j]&0xffff0000u);}
    for(int b=0;b<8;++b){ float xv=in[b*ldin+k]; for(int j=0;j<8;++j) acc[b][j]+=xv*wf[j]; }
  }
  for(int b=0;b<8;++b) for(int j=0;j<8;++j) red[(kg*8+b)*32+c*8+j]=acc[b][j];
  __syncthreads();
  { int b=tid>>5, j=tid&31; float s=0; for(int g=0;g<64;++g) s+=red[(g*8+b)*32+j]; out[b*32+j]=s; }
  __syncthreads();
}
__global__ __launch_bounds__(256) void k_q(const u16* x, const u16* wq, float* q){
  extern __shared__ float sm[];
  float* xin=sm; float* y=sm+8192; float* red=sm+16384; float* out=red+64*8*32;
  int tid=threadIdx.x;
  for(int i=tid;i<8*D;i+=256) xin[i]=bf2f(x[i]);
  __syncthreads();
  ln_rows(xin,y,red,tid);
  __syncthreads();
  gemv32<4096>(y,D,0,D,wq,blockIdx.x*32,red,out,tid);
  { int b=tid>>5, j=tid&31; q[b*4096+blockIdx.x*32+j]=rb(out[b*32+j]); }
}
__global__ __launch_bounds__(256) void k_attn(const float* q, const u16* kc, const u16* vc, float* o){
  __shared__ float qs[128]; __shared__ float sc[1024]; __shared__ float red[256]; __shared__ float op[2][128];
  int b=blockIdx.x>>5, h=blockIdx.x&31, g=h>>3, tid=threadIdx.x;
  if(tid<128) qs[tid]=q[b*4096+h*128+tid];
  __syncthreads();
  const u16* K=kc+((size_t)(b*HKV+g)*SK+(SK-W))*DH;
  const u16* V=vc+((size_t)(b*HKV+g)*SK+(SK-W))*DH;
  float mx=-INFINITY;
  for(int k=tid;k<W;k+=256){
    float s=0;
    for(int d=0;d<128;d+=8){ v4u kv=*(const v4u*)(K+(size_t)k*DH+d);
      for(int j=0;j<4;++j){ s+=qs[d+2*j]*__uint_as_float(kv[j]<<16); s+=qs[d+2*j+1]*__uint_as_float(kv[j]&0xffff0000u);} }
    s=rb(rb(s)*0.08838834764831845f);
    sc[k]=s; mx=fmaxf(mx,s);
  }
  red[tid]=mx; __syncthreads();
  for(int o2=128;o2;o2>>=1){ if(tid<o2) red[tid]=fmaxf(red[tid],red[tid+o2]); __syncthreads(); }
  mx=red[0]; __syncthreads();
  float sum=0;
  for(int k=tid;k<W;k+=256){ float e=__expf(sc[k]-mx); sc[k]=e; sum+=e; }
  red[tid]=sum; __syncthreads();
  for(int o2=128;o2;o2>>=1){ if(tid<o2) red[tid]+=red[tid+o2]; __syncthreads(); }
  sum=red[0];
  for(int k=tid;k<W;k+=256) sc[k]=rb(sc[k]/sum);
  __syncthreads();
  int d=tid&127, half=tid>>7; float acc=0;
  for(int k=half*512;k<half*512+512;++k) acc+=sc[k]*bf2f(V[(size_t)k*DH+d]);
  op[half][d]=acc; __syncthreads();
  if(tid<128) o[b*4096+h*128+tid]=rb(op[0][tid]+op[1][tid]);
}
__global__ __launch_bounds__(256) void k_wo(const float* o, const u16* wo, float* p3){
  extern __shared__ float sm[];
  float* y=sm; float* red=sm+8192; float* out=red+64*8*32;
  int tid=threadIdx.x, ks=blockIdx.y;
  for(int i=tid;i<8*1024;i+=256){ int b=i>>10,k=i&1023; y[i]=o[b*4096+ks*1024+k]; }
  __syncthreads();
  gemv32<1024>(y,1024,ks*1024,1024,wo,blockIdx.x*32,red,out,tid);
  { int b=tid>>5, j=tid&31; p3[(ks*8+b)*1024+blockIdx.x*32+j]=out[b*32+j]; }
}
__global__ __launch_bounds__(256) void k_w1(const u16* x, const float* p3, const u16* w1, float* h1, float* hout){
  extern __shared__ float sm[];
  float* xin=sm; float* y=sm+8192; float* red=sm+16384; float* out=red+64*8*32;
  int tid=threadIdx.x;
  for(int i=tid;i<8*D;i+=256){ float a=rb(p3[i]+p3[8192+i]+p3[16384+i]+p3[24576+i]); float hv=rb(bf2f(x[i])+a); xin[i]=hv; if(blockIdx.x==0) hout[i]=hv; }
  __syncthreads();
  ln_rows(xin,y,red,tid);
  __syncthreads();
  gemv32<DFF>(y,D,0,D,w1,blockIdx.x*32,red,out,tid);
  { int b=tid>>5, j=tid&31; h1[b*DFF+blockIdx.x*32+j]=fmaxf(rb(out[b*32+j]),0.f); }
}
__global__ __launch_bounds__(256) void k_w2(const float* h1, const u16* w2, float* p5){
  extern __shared__ float sm[];
  float* y=sm; float* red=sm+8192; float* out=red+64*8*32;
  int tid=threadIdx.x, ks=blockIdx.y;
  for(int i=tid;i<8*896;i+=256){ int b=i/896,k=i%896; y[b*896+k]=h1[b*DFF+ks*896+k]; }
  __syncthreads();
  gemv32<1024>(y,896,ks*896,896,w2,blockIdx.x*32,red,out,tid);
  { int b=tid>>5, j=tid&31; p5[(ks*8+b)*1024+blockIdx.x*32+j]=out[b*32+j]; }
}
__global__ __launch_bounds__(256) void k_fin(const float* h, const float* p5, u16* out){
  int i=blockIdx.x*256+threadIdx.x;
  float f=rb(p5[i]+p5[8192+i]+p5[16384+i]+p5[24576+i]);
  out[i]=f2bf(h[i]+f);
}
extern "C" void kernel_launch(void* const* d_in, const int* in_sizes, int n_in, void* d_out, int out_size, void* d_ws, size_t ws_size, hipStream_t stream){
  const u16* x=(const u16*)d_in[0]; const u16* kc=(const u16*)d_in[1]; const u16* vc=(const u16*)d_in[2];
  const u16* wq=(const u16*)d_in[3]; const u16* wo=(const u16*)d_in[4]; const u16* w1=(const u16*)d_in[6]; const u16* w2=(const u16*)d_in[7];
  float* ws=(float*)d_ws;
  float* q=ws; float* o=ws+32768; float* p3=ws+65536; float* h1=ws+98304; float* h=ws+131072; float* p5=ws+163840;
  size_t sm1=(16384+64*8*32+256)*4, sm2=(8192+64*8*32+256)*4;
  hipFuncSetAttribute((const void*)k_q, hipFuncAttributeMaxDynamicSharedMemorySize, (int)sm1);
  hipFuncSetAttribute((const void*)k_w1, hipFuncAttributeMaxDynamicSharedMemorySize, (int)sm1);
  hipFuncSetAttribute((const void*)k_wo, hipFuncAttributeMaxDynamicSharedMemorySize, (int)sm2);
  hipFuncSetAttribute((const void*)k_w2, hipFuncAttributeMaxDynamicSharedMemorySize, (int)sm2);
  k_q<<<128,256,sm1,stream>>>(x,wq,q);
  k_attn<<<256,256,0,stream>>>(q,kc,vc,o);
  k_wo<<<dim3(32,4),256,sm2,stream>>>(o,wo,p3);
  k_w1<<<112,256,sm1,stream>>>(x,p3,w1,h1,h);
  k_w2<<<dim3(32,4),256,sm2,stream>>>(h1,w2,p5);
  k_fin<<<32,256,0,stream>>>(h,p5,(u16*)d_out);
}
```

```cpp
#include <hip/hip_runtime.h>
#include <stdint.h>

#define ROLE_OF(c) (((c) & 8) != 0)
namespace mk {
typedef unsigned short u16;
typedef unsigned long long u64;
typedef unsigned v4u __attribute__((ext_vector_type(4)));
typedef unsigned v2u __attribute__((ext_vector_type(2)));
typedef float v4f __attribute__((ext_vector_type(4)));
typedef float v2f __attribute__((ext_vector_type(2)));
typedef short v4s __attribute__((ext_vector_type(4)));
typedef __bf16 v8bf __attribute__((ext_vector_type(8)));
typedef __bf16 v2bf __attribute__((ext_vector_type(2)));
#define LAS __attribute__((address_space(3)))
#define GAS __attribute__((address_space(1)))
typedef __amdgpu_buffer_rsrc_t rsrc_t;

#define W_K 16
#define W_V 32
#define W_1 32
#define W_Q 16
#define W_OA 8
#define N_OA 5
#define W_OB 32
#define W_2 32
#define BAR0_AT 1
#define DRYRUN 1
#define POLL_SLEEP 2

constexpr int NTHR = 13 * 64;
constexpr unsigned SENT = 0xFFFFFFFFu;
constexpr int AUX_SC1 = 16, AUX_NT = 2;
constexpr unsigned SPIN_CAP = 1u << 16;
constexpr int WS_E = 0, WS_RDYX = 1024, WS_RDYY = 2048;
constexpr int WS_Q = 65536, WS_O = WS_Q + 524288, WS_LSE = WS_O + 262144, WS_P3 = WS_LSE + 16384, WS_P4 = WS_P3 + 262144,
              WS_STAT = WS_P4 + 112 * 8 * 640, WS_P5 = WS_STAT + 16384, WS_END = WS_P5 + 32 * 4 * 28 * 128;
constexpr int WS_STAMP = 4 << 20;
constexpr int L_RA = 0, L_RB = 65536, L_M = 131072;
constexpr int L_QIMG = L_M, L_PIMG = L_QIMG + 2048, L_ML = L_PIMG + 4096, L_HIMG = L_ML + 512;
constexpr int L_YIMG = L_M, L_OIMG = L_YIMG + 4096, L_AIMG = L_OIMG + 4096;
constexpr int L_FLAG = L_M + 10240 + 512, L_STAMP = L_FLAG + 128, L_END = L_STAMP + 13 * 16 * 4;
constexpr int F_INIT = 0, F_LN = 1, F_T0 = 2, F_T1 = 3, F_T2 = 4;

struct Params {
  const u16 *x, *kc, *vc, *wq, *wo, *w1, *w2;
  u16* out;
  char* ws;
};

__device__ __forceinline__ float bflo(unsigned u) { return __uint_as_float(u << 16); }
__device__ __forceinline__ float bfhi(unsigned u) { return __uint_as_float(u & 0xffff0000u); }
__device__ __forceinline__ unsigned pk(float a, float b) { v2f f = {a, b}; return __builtin_bit_cast(unsigned, __builtin_convertvector(f, v2bf)); }
__device__ __forceinline__ float rbf(float a) { return bflo(pk(a, a)); }
__device__ __forceinline__ unsigned nosent(unsigned u) { return u < 0xFFFFFFFEu ? u : 0xFFFFFFFEu; }
__device__ __forceinline__ unsigned umax3(unsigned a, unsigned b, unsigned c) { unsigned m = a > b ? a : b; return m > c ? m : c; }
__device__ __forceinline__ unsigned umax4(v4u v, unsigned m) { return umax3(umax3(v[0], v[1], m), v[2], v[3]); }
__device__ __forceinline__ v4f mfma(v4u a, v4u b, v4f c) {
  return __builtin_amdgcn_mfma_f32_16x16x32_bf16(__builtin_bit_cast(v8bf, a), __builtin_bit_cast(v8bf, b), c, 0, 0, 0);
}
__device__ __forceinline__ float rs32(float a, float b) {
  auto s = __builtin_amdgcn_permlane32_swap(__float_as_uint(a), __float_as_uint(b), false, false);
  return __uint_as_float(s[0]) + __uint_as_float(s[1]);
}
__device__ __forceinline__ float rs16(float a, float b) {
  auto s = __builtin_amdgcn_permlane16_swap(__float_as_uint(a), __float_as_uint(b), false, false);
  return __uint_as_float(s[0]) + __uint_as_float(s[1]);
}
__device__ __forceinline__ float mx32(float a) {
  auto s = __builtin_amdgcn_permlane32_swap(__float_as_uint(a), __float_as_uint(a), false, false);
  return fmaxf(__uint_as_float(s[0]), __uint_as_float(s[1]));
}
__device__ __forceinline__ float mx16(float a) {
  auto s = __builtin_amdgcn_permlane16_swap(__float_as_uint(a), __float_as_uint(a), false, false);
  return fmaxf(__uint_as_float(s[0]), __uint_as_float(s[1]));
}
template <int CTRL> __device__ __forceinline__ float dpp(float x) {
  return __builtin_bit_cast(float, __builtin_amdgcn_mov_dpp(__builtin_bit_cast(int, x), CTRL, 0xf, 0xf, true));
}
constexpr int XOR1 = 0xB1, XOR2 = 0x4E, ROR4 = 0x124, ROR8 = 0x128;
__device__ __forceinline__ float wave_sum(float v) {
  v += dpp<XOR1>(v); v += dpp<XOR2>(v); v += dpp<ROR4>(v); v += dpp<ROR8>(v);
  v = rs16(v, v); v = rs32(v, v);
  return v;
}
template <int N> __device__ __forceinline__ void waitvm() { asm volatile("s_waitcnt vmcnt(%0)" ::"i"(N) : "memory"); }
__device__ __forceinline__ void bar() { asm volatile("s_waitcnt lgkmcnt(0)\n\ts_barrier" ::: "memory"); }
__device__ __forceinline__ void lds_set(int off, unsigned v) { asm volatile("ds_write_b32 %0, %1" ::"v"(off), "v"(v) : "memory"); }
__device__ __forceinline__ void lds_wait(LAS char* L, int word, unsigned want) {
  volatile LAS unsigned* f = (volatile LAS unsigned*)(L + L_FLAG) + word;
#pragma unroll 1
  for (unsigned n = 0; n < (SPIN_CAP << 4); ++n) {
    if ((unsigned)__builtin_amdgcn_readfirstlane(*f) >= want) break;
    __builtin_amdgcn_s_sleep(1);
  }
  asm volatile("" ::: "memory");
}
__device__ __forceinline__ v4u ld128(rsrc_t rs, int off) { return __builtin_amdgcn_raw_buffer_load_b128(rs, off, 0, AUX_SC1); }
__device__ __forceinline__ v2u ld64(rsrc_t rs, int off) { return __builtin_amdgcn_raw_buffer_load_b64(rs, off, 0, AUX_SC1); }
__device__ __forceinline__ unsigned ld32(rsrc_t rs, int off) { return __builtin_amdgcn_raw_buffer_load_b32(rs, off, 0, AUX_SC1); }
__device__ __forceinline__ void st128(v4u v, rsrc_t rs, int off) { __builtin_amdgcn_raw_buffer_store_b128(v, rs, off, 0, AUX_SC1); }
__device__ __forceinline__ void st64(v2u v, rsrc_t rs, int off) { __builtin_amdgcn_raw_buffer_store_b64(v, rs, off, 0, AUX_SC1); }
__device__ __forceinline__ void st32(unsigned v, rsrc_t rs, int off) { __builtin_amdgcn_raw_buffer_store_b32(v, rs, off, 0, AUX_SC1); }
constexpr int OOB = 0x40000000;
__device__ __forceinline__ v4u ldsr128(LAS char* L, int off) { return *(LAS v4u*)(L + off); }
__device__ __forceinline__ v4u trfrag(LAS char* L, int off, int half_stride) {
  v4s a = __builtin_amdgcn_ds_read_tr16_b64_v4i16((LAS v4s*)(L + off));
  v4s b = __builtin_amdgcn_ds_read_tr16_b64_v4i16((LAS v4s*)(L + off + half_stride));
  v2u x = __builtin_bit_cast(v2u, a), y = __builtin_bit_cast(v2u, b);
  return v4u{x[0], x[1], y[0], y[1]};
}
__device__ __forceinline__ void acc8(float (&a)[8], v4u v) {
#pragma unroll
  for (int i = 0; i < 4; ++i) { a[2 * i] += bflo(v[i]); a[2 * i + 1] += bfhi(v[i]); }
}
#define ST(k)
#define STL(k)
#define STDUMP()

template <int W, int O0, int O1, int BAR_AT, int FLAG_AT, int FLAG>
__device__ __forceinline__ void stream(rsrc_t rs, LAS char* dst, const int (&vo)[4], int s0, int sa, int sb) {
#pragma unroll 1
  for (int o = O0; o < O1; ++o) {
    if (BAR_AT >= 0 && o == BAR_AT) bar();
    waitvm<W - 4>();
    if (FLAG_AT >= 0 && o == FLAG_AT) lds_set(L_FLAG + FLAG * 4, 1u);
    const int so = s0 + (o >> 1) * sa + (o & 1) * sb;
#pragma unroll
    for (int u = 0; u < 4; ++u)
      __builtin_amdgcn_raw_ptr_buffer_load_lds(rs, (LAS void*)(dst + (o * 4 + u) * 1024), 16, vo[u], so, 0, AUX_NT);
  }
}
__device__ __forceinline__ rsrc_t mkrs(const void* p, int bytes) { return __builtin_amdgcn_make_buffer_rsrc((void*)p, 0, bytes, 0x00020000); }
__device__ __forceinline__ void vo_w256(int (&vo)[4], int lane, int pitch) {
  const int r = lane >> 4, s = lane & 15;
#pragma unroll
  for (int u = 0; u < 4; ++u) vo[u] = (4 * u + r) * pitch + ((((s >> 1) ^ ((4 * u + r) & 7)) << 5) | ((s & 1) << 4));
}
__device__ __forceinline__ void vo_w512(int (&vo)[4], int lane, int pitch) {
  const int r = lane >> 5, s = lane & 31;
#pragma unroll
  for (int u = 0; u < 4; ++u) vo[u] = (2 * u + r) * pitch + (((s >> 4) << 8) | ((((s >> 1) & 7) ^ ((2 * u + r) & 7)) << 5) | ((s & 1) << 4));
}
__device__ __forceinline__ void vo_k(int (&vo)[4], int lane) {
  const int r = lane >> 4, s = lane & 15;
#pragma unroll
  for (int u = 0; u < 4; ++u) vo[u] = (4 * u + r) * 256 + ((s ^ (4 * u + r)) << 4);
}
__device__ __forceinline__ int tr_base(int lane, int pitch, int cb) {
  const int row = lane >> 2;
  return row * pitch + ((cb >> 3) << 8) + (((cb & 7) ^ (row & 7)) << 5) + ((lane & 3) << 3);
}

__device__ __forceinline__ v4f sel4(bool c, v4f a, v4f b) { return v4f{c ? a[0] : b[0], c ? a[1] : b[1], c ? a[2] : b[2], c ? a[3] : b[3]}; }

template <bool isX, int KIND>
__device__ __forceinline__ void role(const Params& p, LAS char* L, const int lane, const int wave) {
  const int xcd = blockIdx.x & 7, jj = blockIdx.x >> 3;
  const int ci = jj >> 1;
  const int cid = xcd * 16 + ci;
  const int id5 = xcd * 14 + ci;

  if constexpr (KIND == 0) {
    int vo[4];
    STL(0);
    if (isX) {
      const int kv0 = ((xcd * 4 + (ci >> 2)) * 4096 + 3072 + (ci & 3) * 256) * 256;
      vo_k(vo, lane);
      stream<W_K, 0, 16, BAR0_AT, -1, 0>(mkrs(p.kc, 8 * 4 * 4096 * 256), L + L_RA, vo, kv0, 8192, 4096);
      STL(1);
      vo_w256(vo, lane, 256);
      stream<W_V, 0, 16, -1, (W_V - 4 + 3) / 4, F_T0>(mkrs(p.vc, 8 * 4 * 4096 * 256), L + L_RB, vo, kv0, 8192, 4096);
      STL(2);
      bar();
      waitvm<0>();
      STL(3);
      bar();
      if (ci < 14) {
        vo_w512(vo, lane, 7168);
        stream<W_1, 0, 16, -1, -1, 0>(mkrs(p.w1, 1024 * 7168), L + L_RA, vo, xcd * 128 * 7168 + ci * 512, 16 * 7168, 8 * 7168);
        STL(4);
        waitvm<0>();
        lds_set(L_FLAG + F_T2 * 4, 1u);
        STL(5);
      }
      bar();
    } else {
      vo_w256(vo, lane, 8192);
      stream<W_Q, 0, 16, BAR0_AT, -1, 0>(mkrs(p.wq, 1024 * 8192), L + L_RA, vo, (cid & 3) * 256 * 8192 + (cid >> 2) * 256, 32 * 8192, 16 * 8192);
      STL(1);
      vo_w256(vo, lane, 2048);
      const int rs3 = cid >> 3;
      const int wo0 = ((rs3 >> 2) * 1024 + (rs3 & 3) * 32) * 2048 + (cid & 7) * 256;
      const rsrc_t rwo = mkrs(p.wo, 4096 * 2048);
      stream<W_OA, 0, N_OA, -1, (W_OA - 4 + 3) / 4, F_T0>(rwo, L + L_RB, vo, wo0, 128 * 2048, 16 * 2048);
      STL(2);
      stream<W_OB, N_OA, 16, -1, -1, 0>(rwo, L + L_RB, vo, wo0, 128 * 2048, 16 * 2048);
      waitvm<0>();
      lds_set(L_FLAG + F_T1 * 4, 1u);
      STL(3);
      bar();
      if (ci < 14) {
        vo_w512(vo, lane, 2048);
        stream<W_2, 0, 16, -1, -1, 0>(mkrs(p.w2, 3584 * 2048), L + L_RA, vo, (id5 >> 2) * 128 * 2048 + (id5 & 3) * 512, 16 * 2048, 8 * 2048);
        STL(4);
        waitvm<0>();
        lds_set(L_FLAG + F_T2 * 4, 1u);
        STL(5);
        bar();
      }
    }
    STDUMP();
    return;
  }

  const rsrc_t rsw = mkrs(p.ws, WS_END);
  const v2u ep = ld64(rsw, WS_E);
  const int b16 = lane & 15, g4 = lane >> 4, hw = lane >> 5;
  const int fr_off = g4 * 128 + (lane & 7) * 16;

  if constexpr (KIND == 1) {
    const int mw = wave - 1;
    const v4u sv = {SENT, SENT, SENT, SENT};
    if (isX) {
      if (lane < 8) *(LAS v4u*)(L + L_HIMG + mw * 640 + 512 + lane * 16) = v4u{0x3F803F80u, 0x3F803F80u, 0x3F803F80u, 0x3F803F80u};
      const int g = ci >> 2, s = ci & 3;
      int a0 = WS_O + (((g * 4 + mw) * 8 + xcd) * 4 + s) * 512 + lane * 16;
      if (lane >= 32) a0 = (lane < 40 && mw < 2) ? (mw == 0 ? WS_LSE + ((g * 8 + xcd) * 4 + s) * 128 : WS_STAT + (ci * 8 + xcd) * 128) + (lane - 32) * 16 : OOB;
      st128(sv, rsw, a0);
      const int a1 = (lane < 40 && ci < 14) ? WS_P4 + ((ci * 8 + 2 * mw) * 8 + xcd) * 640 + lane * 16 : OOB;
      st128(sv, rsw, a1);
      st128(sv, rsw, a1 + 8 * 640);
    } else {
      st128(sv, rsw, WS_Q + (((2 * mw + hw) * 32 + (cid >> 2)) * 4 + (cid & 3)) * 512 + (lane & 31) * 16);
      st128(sv, rsw, lane < 32 ? WS_P3 + (((cid & 7) * 4 + mw) * 16 + (cid >> 3)) * 512 + lane * 16 : OOB);
      st128(sv, rsw, ci < 14 ? WS_P5 + ((((id5 & 3) * 8 + 2 * mw + hw) * 4 + ((lane >> 3) & 3)) * 28 + (id5 >> 2)) * 128 + (lane & 7) * 16 : OOB);
    }
    waitvm<0>();
    {
      unsigned old = 0;
      if (lane == 0) old = __hip_atomic_fetch_add((LAS unsigned*)(L + L_FLAG) + F_INIT, 1u, __ATOMIC_RELAXED, __HIP_MEMORY_SCOPE_WORKGROUP);
      old = __builtin_amdgcn_readfirstlane(old);
      if (old == 3u) {
        const u64 tag = (((u64)ep[1] << 32) | ep[0]) + 1ull;
        st64(v2u{(unsigned)tag, (unsigned)(tag >> 32)}, rsw, lane == 0 ? (isX ? WS_RDYX : WS_RDYY) + cid * 8 : OOB);
      }
    }
#pragma unroll 1
    for (int it0 = DRYRUN ? 0 : 1; it0 < 2; ++it0) {
      int it = it0;
      asm volatile("" : "+s"(it));
      const bool dry = it == 0;
      const rsrc_t rs = mkrs(p.ws, dry ? 0 : WS_END);
      if (isX) {
        if (!dry) { bar(); bar(); }
        if (!dry && ci >= 14) { bar(); break; }
        if (!dry) lds_wait(L, F_T2, 1u);
        ST(0);
        v4u a[4][4];
#pragma unroll
        for (int c = 0; c < 4; ++c) {
          const int tb = L_RA + tr_base(lane, 512, 4 * mw + c);
#pragma unroll
          for (int t = 0; t < 4; ++t) a[c][t] = trfrag(L, tb + t * 32 * 512, 16 * 512);
        }
        ST(1);
        if (!dry) bar();
        ST(2);
        const int ho = L_HIMG + (b16 < 8 ? fr_off : 512);
        v4f acc[4] = {};
#pragma unroll
        for (int t = 0; t < 4; ++t) {
          const v4u bf = ldsr128(L, ho + t * 640);
#pragma unroll
          for (int c = 0; c < 4; ++c) acc[c] = mfma(a[c][t], bf, acc[c]);
        }
        const int po = b16 < 8 ? b16 * 64 + g4 * 16 : (b16 < 10 ? 512 + (b16 - 8) * 64 + g4 * 16 : OOB);
#pragma unroll
        for (int f = 0; f < 2; ++f) {
          const v4f c0 = acc[2 * f], c1 = acc[2 * f + 1], cs = sel4(b16 == 8, c0, c1);
          v4u o = {pk(c0[0], c0[1]), pk(c0[2], c0[3]), pk(c1[0], c1[1]), pk(c1[2], c1[3])};
          if (b16 >= 8) o = __builtin_bit_cast(v4u, cs);
          o = v4u{nosent(o[0]), nosent(o[1]), nosent(o[2]), nosent(o[3])};
          st128(o, rs, WS_P4 + ((ci * 8 + 2 * mw + f) * 8 + xcd) * 640 + po);
        }
        ST(3);
      } else {
#pragma unroll 1
        for (int r = 0; r < 2; ++r) {
          if (dry && r != (mw & 1)) continue;
          if (dry && mw >= 2) continue;
          if (!dry) lds_wait(L, r == 0 ? F_T0 : F_T1, 1u);
          ST(r * 4 + 0);
          v4u a[2][8];
#pragma unroll
          for (int c = 0; c < 2; ++c) {
            const int tb = (r == 0 ? L_RA : L_RB) + tr_base(lane, 256, 2 * mw + c);
#pragma unroll
            for (int t = 0; t < 8; ++t) a[c][t] = trfrag(L, tb + t * 32 * 256, 16 * 256);
          }
          ST(r * 4 + 1);
          if (!dry) {
            if (r == 0) { lds_wait(L, F_LN, 8u); lds_wait(L, F_INIT, 4u); }
            else bar();
          }
          ST(r * 4 + 2);
          const int bo = (r == 0 ? L_YIMG : L_OIMG) + fr_off;
          v4f acc[2] = {};
#pragma unroll
          for (int t = 0; t < 8; ++t) {
            const v4u bf = ldsr128(L, bo + t * 512);
#pragma unroll
            for (int c = 0; c < 2; ++c) acc[c] = mfma(a[c][t], bf, acc[c]);
          }
          v4u o; int off;
          if (r == 0) {
            o = __builtin_bit_cast(v4u, sel4(b16 < 8, acc[0], acc[1]));
            off = WS_Q + (((lane & 7) * 32 + (cid >> 2)) * 4 + (cid & 3)) * 512 + mw * 128 + (b16 >> 3) * 64 + g4 * 16;
          } else {
            o = v4u{pk(acc[0][0], acc[0][1]), pk(acc[0][2], acc[0][3]), pk(acc[1][0], acc[1][1]), pk(acc[1][2], acc[1][3])};
            off = b16 < 8 ? WS_P3 + (((cid & 7) * 4 + mw) * 16 + (cid >> 3)) * 512 + b16 * 64 + g4 * 16 : OOB;
          }
          o = v4u{nosent(o[0]), nosent(o[1]), nosent(o[2]), nosent(o[3])};
          st128(o, rs, off);
          ST(r * 4 + 3);
        }
        if (!dry && ci >= 14) break;
        if (dry && mw < 2) continue;
        if (!dry) lds_wait(L, F_T2, 1u);
        ST(8);
        v4u a[4][4];
#pragma unroll
        for (int c = 0; c < 4; ++c) {
          const int tb = L_RA + tr_base(lane, 512, 4 * mw + c);
#pragma unroll
          for (int t = 0; t < 4; ++t) a[c][t] = trfrag(L, tb + t * 32 * 512, 16 * 512);
        }
        ST(9);
        if (!dry) bar();
        ST(10);
        v4f acc[4] = {};
#pragma unroll
        for (int t = 0; t < 4; ++t) {
          const v4u bf = ldsr128(L, L_AIMG + fr_off + t * 512);
#pragma unroll
          for (int c = 0; c < 4; ++c) acc[c] = mfma(a[c][t], bf, acc[c]);
        }
        const v4f c0 = sel4(b16 < 8, acc[0], acc[2]), c1 = sel4(b16 < 8, acc[1], acc[3]);
        v4u o = {nosent(pk(c0[0], c0[1])), nosent(pk(c0[2], c0[3])), nosent(pk(c1[0], c1[1])), nosent(pk(c1[2], c1[3]))};
        st128(o, rs, WS_P5 + ((((id5 & 3) * 8 + 2 * mw + (b16 >> 3)) * 4 + ((lane & 7) >> 1)) * 28 + (id5 >> 2)) * 128 + (lane & 1) * 64 + g4 * 16);
        ST(11);
      }
    }
    STDUMP();
    return;
  }

  const int gw = wave - 5;
  const int gf = gw & 3, gbh = gw >> 2;
  const int gb = 4 * gbh + ((lane >> 2) & 3);
  const int gk = 16 * hw + 4 * (lane & 3) + 2 * (g4 & 1);
  const int gimg = (lane & 3) * 128 + gb * 16 + hw * 8 + (g4 & 1) * 4;
  const int gpo = gb * 64 + (lane & 3) * 16;
  unsigned xpair = 0;
  if (isX) {
    xpair = *(const GAS unsigned*)(p.x + gb * 1024 + xcd * 128 + gf * 32 + gk);
  } else {
    const GAS u16* xr = (const GAS u16*)p.x + gw * 1024;
    const v4u x0 = *(const GAS v4u*)(xr + lane * 8), x1 = *(const GAS v4u*)(xr + 512 + lane * 8);
    const v2u xq = *(const GAS v2u*)(xr + (cid & 3) * 256 + lane * 4);
    float s = 0.f;
#pragma unroll
    for (int i = 0; i < 4; ++i) s += (bflo(x0[i]) + bfhi(x0[i])) + (bflo(x1[i]) + bfhi(x1[i]));
    const float mean = wave_sum(s) * (1.f / 1024.f);
    float q = 0.f;
#pragma unroll
    for (int i = 0; i < 4; ++i) {
      const float d0 = bflo(x0[i]) - mean, d1 = bfhi(x0[i]) - mean, d2 = bflo(x1[i]) - mean, d3 = bfhi(x1[i]) - mean;
      q += (d0 * d0 + d1 * d1) + (d2 * d2 + d3 * d3);
    }
    const float var = rbf(wave_sum(q) * (1.f / 1024.f)), m = rbf(mean);
    const float den = rbf(sqrtf(rbf(var + 1e-5f)));
    const v2u y = {pk(rbf(bflo(xq[0]) - m) / den, rbf(bfhi(xq[0]) - m) / den), pk(rbf(bflo(xq[1]) - m) / den, rbf(bfhi(xq[1]) - m) / den)};
    *(LAS v2u*)(L + L_YIMG + (lane >> 3) * 512 + (lane & 3) * 128 + gw * 16 + ((lane >> 2) & 1) * 8) = y;
    asm volatile("s_waitcnt lgkmcnt(0)" ::: "memory");
    if (lane == 0) __hip_atomic_fetch_add((LAS unsigned*)(L + L_FLAG) + F_LN, 1u, __ATOMIC_RELAXED, __HIP_MEMORY_SCOPE_WORKGROUP);
  }
  const u64 tag = (((u64)ep[1] << 32) | ep[0]) + 1ull;
#pragma unroll 1
  for (int it0 = DRYRUN ? 0 : 1; it0 < 2; ++it0) {
    int it = it0;
    asm volatile("" : "+s"(it));
    const bool dry = it == 0;
    const rsrc_t rs = mkrs(p.ws, dry ? 0 : WS_END);
    if (!dry) {
      ST(0);
      _Pragma("unroll 1") for (unsigned n = 0; n < SPIN_CAP; ++n) {
        const v4u r = ld128(rs, (isX ? WS_RDYY : WS_RDYX) + lane * 16);
        const bool ok = r[0] == (unsigned)tag && r[1] == (unsigned)(tag >> 32) && r[2] == (unsigned)tag && r[3] == (unsigned)(tag >> 32);
        if (__all(ok)) break;
        __builtin_amdgcn_s_sleep(8);
      }
      __builtin_amdgcn_fence(__ATOMIC_ACQUIRE, "wavefront");
      ST(1);
    }
    if (isX) {
      const int g = ci >> 2, sp = ci & 3;
      if (!dry || (gw & 3) == 0) {
        if (!dry) lds_wait(L, F_T0, 1u);
        v4u kf[2][4];
#pragma unroll
        for (int t = 0; t < 4; ++t) {
          const int ko = L_RA + (32 * gw + b16) * 256 + (((4 * t + g4) ^ b16) << 4);
          kf[0][t] = ldsr128(L, ko);
          kf[1][t] = ldsr128(L, ko + 4096);
        }
        ST(2);
        const int qo = WS_Q + ((xcd * 32 + g * 8 + gw) * 4) * 512 + lane * 16;
        v4u q0, q1;
        _Pragma("unroll 1") for (unsigned n = 0; n < SPIN_CAP; ++n) {
          q0 = ld128(rs, qo); q1 = ld128(rs, qo + 1024);
          if (__all(umax4(q0, umax4(q1, 0u)) != SENT)) break;
          __builtin_amdgcn_s_sleep(POLL_SLEEP);
        }
        ST(3);
        const v4f qs = __builtin_bit_cast(v4f, q0) + __builtin_bit_cast(v4f, q1);
        const int d = 4 * (lane & 31) + 2 * hw;
        *(LAS unsigned*)(L + L_QIMG + (d >> 5) * 512 + ((d >> 3) & 3) * 128 + gw * 16 + (d & 7) * 2) = pk(rs32(qs[0], qs[2]), rs32(qs[1], qs[3]));
        if (!dry) bar();
        ST(4);
        v4f sc[2] = {};
#pragma unroll
        for (int t = 0; t < 4; ++t) {
          const v4u qf = ldsr128(L, L_QIMG + fr_off + t * 512);
          sc[0] = mfma(kf[0][t], qf, sc[0]);
          sc[1] = mfma(kf[1][t], qf, sc[1]);
        }
        float s[8];
#pragma unroll
        for (int i = 0; i < 4; ++i) {
          const unsigned r1 = pk(sc[i >> 1][2 * (i & 1)], sc[i >> 1][2 * (i & 1) + 1]);
          const unsigned r2 = pk(bflo(r1) * 0.08838834764831845f, bfhi(r1) * 0.08838834764831845f);
          s[2 * i] = bflo(r2); s[2 * i + 1] = bfhi(r2);
        }
        float m = fmaxf(fmaxf(fmaxf(s[0], s[1]), fmaxf(s[2], s[3])), fmaxf(fmaxf(s[4], s[5]), fmaxf(s[6], s[7])));
        m = mx32(mx16(m));
        float l = 0.f;
#pragma unroll
        for (int i = 0; i < 8; ++i) { s[i] = __builtin_amdgcn_exp2f((s[i] - m) * 1.4426950408889634f); l += s[i]; }
        l = rs16(l, l); l = rs32(l, l);
        if (b16 < 8) {
          *(LAS v4u*)(L + L_PIMG + gw * 512 + fr_off) = v4u{pk(s[0], s[1]), pk(s[2], s[3]), pk(s[4], s[5]), pk(s[6], s[7])};
          if (g4 == 0) *(LAS v2f*)(L + L_ML + (b16 * 8 + gw) * 8) = v2f{m * 1.4426950408889634f, l};
        }
        ST(5);
        if (!dry) bar();
        ST(6);
      }
      if (!dry || (gw & 3) == 1) {
        const int tb = L_RB + tr_base(lane, 256, gw);
        v4f d[8];
        v4f ml[4];
#pragma unroll
        for (int t = 0; t < 4; ++t) ml[t] = *(LAS v4f*)(L + L_ML + (lane & 7) * 64 + t * 16);
#pragma unroll
        for (int t = 0; t < 8; ++t) d[t] = mfma(trfrag(L, tb + t * 32 * 256, 16 * 256), ldsr128(L, L_PIMG + fr_off + t * 512), v4f{0.f, 0.f, 0.f, 0.f});
        float M = fmaxf(fmaxf(fmaxf(ml[0][0], ml[0][2]), fmaxf(ml[1][0], ml[1][2])), fmaxf(fmaxf(ml[2][0], ml[2][2]), fmaxf(ml[3][0], ml[3][2])));
        float f[8], Ls = 0.f;
#pragma unroll
        for (int t = 0; t < 8; ++t) { f[t] = __builtin_amdgcn_exp2f(ml[t >> 1][2 * (t & 1)] - M); Ls += f[t] * ml[t >> 1][2 * (t & 1) + 1]; }
        if (gw == 0 || dry) st32(nosent(__float_as_uint(M + __builtin_amdgcn_logf(Ls))), rs, lane < 8 ? WS_LSE + ((g * 8 + xcd) * 4 + sp) * 128 + lane * 4 : OOB);
        const float inv = __builtin_amdgcn_rcpf(Ls);
        v4f o = d[0] * f[0];
#pragma unroll
        for (int t = 1; t < 8; ++t) o += d[t] * f[t];
        o *= inv;
        st64(v2u{nosent(pk(o[0], o[1])), nosent(pk(o[2], o[3]))}, rs,
             b16 < 8 ? WS_O + (((g * 4 + (gw >> 1)) * 8 + xcd) * 4 + sp) * 512 + (gw & 1) * 256 + b16 * 32 + g4 * 8 : OOB);
        ST(7);
      }
      if (!dry || (gw & 3) == 2) {
        const int po = WS_P3 + ((xcd * 4 + gf) * 16 + g4) * 512 + gpo;
        v4u v[4];
        _Pragma("unroll 1") for (unsigned n = 0; n < SPIN_CAP; ++n) {
#pragma unroll
          for (int i = 0; i < 4; ++i) v[i] = ld128(rs, po + i * 2048);
          if (__all(umax4(v[0], umax4(v[1], umax4(v[2], umax4(v[3], 0u)))) != SENT)) break;
          __builtin_amdgcn_s_sleep(POLL_SLEEP);
        }
        ST(8);
        float a[8] = {};
#pragma unroll
        for (int i = 0; i < 4; ++i) acc8(a, v[i]);
        const float r0 = rs32(a[0], a[4]), r1 = rs32(a[1], a[5]), r2 = rs32(a[2], a[6]), r3 = rs32(a[3], a[7]);
        const unsigned at = pk(rs16(r0, r2), rs16(r1, r3));
        *(LAS unsigned*)(L + L_HIMG + gf * 640 + gimg) = pk(bflo(xpair) + bflo(at), bfhi(xpair) + bfhi(at));
        if (!dry) bar();
        ST(9);
      }
      if (dry ? gw == 3 : gw == 0) {
        const int ho = L_HIMG + g4 * 640 + ((lane >> 3) & 1) * 256 + (lane & 7) * 16;
        const v4u h0 = ldsr128(L, ho), h1 = ldsr128(L, ho + 128);
        float s = 0.f, q = 0.f;
#pragma unroll
        for (int i = 0; i < 4; ++i) {
          const float e0 = bflo(h0[i]), e1 = bfhi(h0[i]), e2 = bflo(h1[i]), e3 = bfhi(h1[i]);
          s += (e0 + e1) + (e2 + e3);
          q += (e0 * e0 + e1 * e1) + (e2 * e2 + e3 * e3);
        }
        s += dpp<ROR8>(s); q += dpp<ROR8>(q);
        s = rs16(s, s); q = rs16(q, q);
        s = rs32(s, s); q = rs32(q, q);
        st64(v2u{nosent(__float_as_uint(s)), nosent(__float_as_uint(q))}, rs, (lane < 8 && ci < 14) ? WS_STAT + (ci * 8 + xcd) * 128 + lane * 8 : OOB);
        ST(10);
      }
      if (dry ? gw == 7 : gw == 1) {
        const int f = ci >> 2, jb = ci & 3;
        const int ob = 2 * jb + ((lane >> 2) & 1), og = lane & 3, oj = 4 * hw + 2 * (g4 & 1) + ((lane >> 3) & 1);
        const float hv = bflo(*(LAS u16*)(L + L_HIMG + f * 640 + og * 128 + ob * 16 + oj * 2));
        const int po = WS_P5 + (((xcd * 4 + f) * 4 + jb) * 28) * 128 + lane * 16;
        v4u v[4];
        _Pragma("unroll 1") for (unsigned n = 0; n < SPIN_CAP; ++n) {
#pragma unroll
          for (int i = 0; i < 3; ++i) v[i] = ld128(rs, po + i * 1024);
          v[3] = ld128(rs, lane < 32 ? po + 3072 : OOB);
          if (__all(umax4(v[0], umax4(v[1], umax4(v[2], umax4(v[3], 0u)))) != SENT)) break;
          __builtin_amdgcn_s_sleep(POLL_SLEEP);
        }
        ST(11);
        float a[8] = {};
#pragma unroll
        for (int i = 0; i < 4; ++i) acc8(a, v[i]);
        const float r0 = rs32(a[0], a[4]), r1 = rs32(a[1], a[5]), r2 = rs32(a[2], a[6]), r3 = rs32(a[3], a[7]);
        float u0 = rs16(r0, r2), u1 = rs16(r1, r3);
        u0 += dpp<ROR8>(u0); u1 += dpp<ROR8>(u1);
        const float ff = rbf((lane & 8) ? u1 : u0);
        const rsrc_t ro = mkrs(p.out, dry ? 0 : 16384);
        __builtin_amdgcn_raw_buffer_store_b16((u16)pk(hv + ff, 0.f), ro, (ob * 1024 + xcd * 128 + f * 32 + 16 * (oj >> 2) + 4 * og + (oj & 3)) * 2, 0, 0);
        if (cid == 0) st64(v2u{(unsigned)tag, (unsigned)(tag >> 32)}, rs, lane == 0 ? WS_E : OOB);
        ST(12);
      }
    } else {
      if (!dry || (gw & 1) == 0) {
        const int rs3 = cid >> 3;
        const int oo = WS_O + ((rs3 * 8 + gw) * 4) * 512 + lane * 16;
        const int lo = WS_LSE + (((rs3 >> 2) * 8 + gw) * 4 + hw) * 128 + ((lane >> 1) & 7) * 4;
        v4u v0, v1; unsigned e0, e1;
        _Pragma("unroll 1") for (unsigned n = 0; n < SPIN_CAP; ++n) {
          e0 = ld32(rs, lo); e1 = ld32(rs, lo + 256);
          v0 = ld128(rs, oo); v1 = ld128(rs, oo + 1024);
          if (__all(umax4(v0, umax4(v1, e0 > e1 ? e0 : e1)) != SENT)) break;
          __builtin_amdgcn_s_sleep(POLL_SLEEP);
        }
        ST(2);
        const float l0 = __uint_as_float(e0), l1 = __uint_as_float(e1);
        const float M = mx32(fmaxf(l0, l1));
        float w0 = __builtin_amdgcn_exp2f(l0 - M), w1 = __builtin_amdgcn_exp2f(l1 - M);
        const float ws_ = w0 + w1;
        const float inv = __builtin_amdgcn_rcpf(rs32(ws_, ws_));
        w0 *= inv; w1 *= inv;
        float a[8];
#pragma unroll
        for (int i = 0; i < 4; ++i) { a[2 * i] = w0 * bflo(v0[i]) + w1 * bflo(v1[i]); a[2 * i + 1] = w0 * bfhi(v0[i]) + w1 * bfhi(v1[i]); }
        const v2u o = {pk(rs32(a[0], a[4]), rs32(a[1], a[5])), pk(rs32(a[2], a[6]), rs32(a[3], a[7]))};
        *(LAS v2u*)(L + L_OIMG + ((lane >> 1) & 7) * 512 + (2 * (lane & 1) + hw) * 128 + gw * 16 + (g4 & 1) * 8) = o;
        if (!dry) bar();
        ST(3);
      }
      if (!dry && ci >= 14) break;
      if (!dry || (gw & 1) == 1) {
        const int F = (id5 >> 2) * 4 + gf;
        const int po = WS_P4 + (F * 8 + g4) * 640 + gpo;
        const int co = WS_P4 + (F * 8 + 2 * ((lane >> 2) & 3)) * 640 + 512 + gk * 4;
        const int so = WS_STAT + ((id5 >> 3) * 8 + (lane & 3) + 4 * (g4 & 1)) * 128 + gb * 8;
        v4u v0, v1; v2u c0, c1, sq;
        _Pragma("unroll 1") for (unsigned n = 0; n < SPIN_CAP; ++n) {
          sq = ld64(rs, so); c0 = ld64(rs, co); c1 = ld64(rs, co + 640);
          v0 = ld128(rs, po); v1 = ld128(rs, po + 4 * 640);
          if (__all(umax4(v0, umax4(v1, umax3(umax3(sq[0], sq[1], c0[0]), umax3(c0[1], c1[0], c1[1]), 0u))) != SENT)) break;
          __builtin_amdgcn_s_sleep(POLL_SLEEP);
        }
        ST(4);
        float s = __uint_as_float(sq[0]), q = __uint_as_float(sq[1]);
        s += dpp<XOR1>(s); q += dpp<XOR1>(q); s += dpp<XOR2>(s); q += dpp<XOR2>(q);
        s = rs16(s, s); q = rs16(q, q);
        const float mean = s * (1.f / 1024.f);
        const float var = rbf(fmaxf(q * (1.f / 1024.f) - mean * mean, 0.f)), m = rbf(mean);
        const float inv = __builtin_amdgcn_rcpf(rbf(__builtin_amdgcn_sqrtf(rbf(var + 1e-5f))));
        float ca = __uint_as_float(c0[0]) + __uint_as_float(c1[0]), cb = __uint_as_float(c0[1]) + __uint_as_float(c1[1]);
        ca += dpp<ROR8>(ca); cb += dpp<ROR8>(cb); ca += dpp<ROR4>(ca); cb += dpp<ROR4>(cb);
        float a[8] = {};
        acc8(a, v0); acc8(a, v1);
        const float r0 = rs32(a[0], a[4]), r1 = rs32(a[1], a[5]), r2 = rs32(a[2], a[6]), r3 = rs32(a[3], a[7]);
        const float u0 = (rs16(r0, r2) - m * ca) * inv, u1 = (rs16(r1, r3) - m * cb) * inv;
        *(LAS unsigned*)(L + L_AIMG + gf * 512 + gimg) = pk(u0 < 0.f ? 0.f : u0, u1 < 0.f ? 0.f : u1);
        if (!dry) bar();
        ST(5);
      }
    }
  }
  STDUMP();
}

__global__ void __launch_bounds__(NTHR) fused(Params p) {
  extern __shared__ __attribute__((aligned(16))) char smem[];
  LAS char* L = (LAS char*)smem;
  const int lane = threadIdx.x & 63, wave = __builtin_amdgcn_readfirstlane(threadIdx.x >> 6);
  const bool isX = (blockIdx.x & 8) == 0;
  if (__builtin_expect(wave == 0, 1)) {
    if (isX) { role<true, 0>(p, L, lane, wave); __builtin_amdgcn_endpgm(); }
    role<false, 0>(p, L, lane, wave); __builtin_amdgcn_endpgm();
  }
  if (wave == 1) lds_set(L_FLAG + (lane & 15) * 4, 0u);
  bar();
  if (wave <= 4) {
    if (isX) { role<true, 1>(p, L, lane, wave); __builtin_amdgcn_endpgm(); }
    role<false, 1>(p, L, lane, wave); __builtin_amdgcn_endpgm();
  }
  if (isX) { role<true, 2>(p, L, lane, wave); __builtin_amdgcn_endpgm(); }
  role<false, 2>(p, L, lane, wave);
}
}

extern "C" void kernel_launch(void* const* d_in, const int* in_sizes, int n_in, void* d_out, int out_size, void* d_ws, size_t ws_size,
                              hipStream_t stream) {
  mk::Params p;
  p.x = (const mk::u16*)d_in[0]; p.kc = (const mk::u16*)d_in[1]; p.vc = (const mk::u16*)d_in[2];
  p.wq = (const mk::u16*)d_in[3]; p.wo = (const mk::u16*)d_in[4]; p.w1 = (const mk::u16*)d_in[6]; p.w2 = (const mk::u16*)d_in[7];
  p.out = (mk::u16*)d_out; p.ws = (char*)d_ws;
  (void)hipFuncSetAttribute((const void*)mk::fused, hipFuncAttributeMaxDynamicSharedMemorySize, mk::L_END);
  mk::fused<<<256, mk::NTHR, mk::L_END, stream>>>(p);
}
```

```cpp
#include <hip/hip_runtime.h>
#include <stdint.h>

#define ROLE_OF(c) (((c) & 8) != 0)
namespace mk {
typedef unsigned short u16;
typedef unsigned long long u64;
typedef unsigned v4u __attribute__((ext_vector_type(4)));
typedef unsigned v2u __attribute__((ext_vector_type(2)));
typedef float v4f __attribute__((ext_vector_type(4)));
typedef float v2f __attribute__((ext_vector_type(2)));
typedef short v4s __attribute__((ext_vector_type(4)));
typedef __bf16 v8bf __attribute__((ext_vector_type(8)));
typedef __bf16 v2bf __attribute__((ext_vector_type(2)));
#define LAS __attribute__((address_space(3)))
#define GAS __attribute__((address_space(1)))
typedef __amdgpu_buffer_rsrc_t rsrc_t;

#define W_K 16
#define W_V 32
#define W_1 32
#define W_Q 16
#define W_OA 8
#define N_OA 5
#define W_OB 32
#define W_2 32
#define BAR0_AT 1
#define DRYRUN 1
#define POLL_SLEEP 2

constexpr int NTHR = 13 * 64;
constexpr unsigned SENT = 0xFFFFFFFFu;
constexpr int AUX_SC1 = 16, AUX_NT = 2;
constexpr unsigned SPIN_CAP = 1u << 16;
constexpr int WS_E = 0, WS_RDYX = 1024, WS_RDYY = 2048;
constexpr int WS_Q = 65536, WS_O = WS_Q + 524288, WS_LSE = WS_O + 262144, WS_P3 = WS_LSE + 16384, WS_P4 = WS_P3 + 262144,
              WS_STAT = WS_P4 + 112 * 8 * 640, WS_P5 = WS_STAT + 16384, WS_END = WS_P5 + 32 * 4 * 28 * 128;
constexpr int WS_STAMP = 4 << 20;
constexpr int L_RA = 0, L_RB = 65536, L_M = 131072;
constexpr int L_QIMG = L_M, L_PIMG = L_QIMG + 2048, L_ML = L_PIMG + 4096, L_HIMG = L_ML + 512;
constexpr int L_YIMG = L_M, L_OIMG = L_YIMG + 4096, L_AIMG = L_OIMG + 4096;
constexpr int L_FLAG = L_M + 10240 + 512, L_STAMP = L_FLAG + 128, L_END = L_STAMP + 13 * 16 * 4;
constexpr int F_INIT = 0, F_LN = 1, F_T0 = 2, F_T1 = 3, F_T2 = 4;

struct Params {
  const u16 *x, *kc, *vc, *wq, *wo, *w1, *w2;
  u16* out;
  char* ws;
};

__device__ __forceinline__ float bflo(unsigned u) { return __uint_as_float(u << 16); }
__device__ __forceinline__ float bfhi(unsigned u) { return __uint_as_float(u & 0xffff0000u); }
__device__ __forceinline__ unsigned pk(float a, float b) { v2f f = {a, b}; return __builtin_bit_cast(unsigned, __builtin_convertvector(f, v2bf)); }
__device__ __forceinline__ float rbf(float a) { return bflo(pk(a, a)); }
__device__ __forceinline__ unsigned nosent(unsigned u) { return u < 0xFFFFFFFEu ? u : 0xFFFFFFFEu; }
__device__ __forceinline__ unsigned umax3(unsigned a, unsigned b, unsigned c) { unsigned m = a > b ? a : b; return m > c ? m : c; }
__device__ __forceinline__ unsigned umax4(v4u v, unsigned m) { return umax3(umax3(v[0], v[1], m), v[2], v[3]); }
__device__ __forceinline__ v4f mfma(v4u a, v4u b, v4f c) {
  return __builtin_amdgcn_mfma_f32_16x16x32_bf16(__builtin_bit_cast(v8bf, a), __builtin_bit_cast(v8bf, b), c, 0, 0, 0);
}
__device__ __forceinline__ float rs32(float a, float b) {
  auto s = __builtin_amdgcn_permlane32_swap(__float_as_uint(a), __float_as_uint(b), false, false);
  return __uint_as_float(s[0]) + __uint_as_float(s[1]);
}
__device__ __forceinline__ float rs16(float a, float b) {
  auto s = __builtin_amdgcn_permlane16_swap(__float_as_uint(a), __float_as_uint(b), false, false);
  return __uint_as_float(s[0]) + __uint_as_float(s[1]);
}
__device__ __forceinline__ float mx32(float a) {
  auto s = __builtin_amdgcn_permlane32_swap(__float_as_uint(a), __float_as_uint(a), false, false);
  return fmaxf(__uint_as_float(s[0]), __uint_as_float(s[1]));
}
__device__ __forceinline__ float mx16(float a) {
  auto s = __builtin_amdgcn_permlane16_swap(__float_as_uint(a), __float_as_uint(a), false, false);
  return fmaxf(__uint_as_float(s[0]), __uint_as_float(s[1]));
}
template <int CTRL> __device__ __forceinline__ float dpp(float x) {
  return __builtin_bit_cast(float, __builtin_amdgcn_mov_dpp(__builtin_bit_cast(int, x), CTRL, 0xf, 0xf, true));
}
constexpr int XOR1 = 0xB1, XOR2 = 0x4E, ROR4 = 0x124, ROR8 = 0x128;
__device__ __forceinline__ float wave_sum(float v) {
  v += dpp<XOR1>(v); v += dpp<XOR2>(v); v += dpp<ROR4>(v); v += dpp<ROR8>(v);
  v = rs16(v, v); v = rs32(v, v);
  return v;
}
template <int N> __device__ __forceinline__ void waitvm() { asm volatile("s_waitcnt vmcnt(%0)" ::"i"(N) : "memory"); }
__device__ __forceinline__ void bar() { asm volatile("s_waitcnt lgkmcnt(0)\n\ts_barrier" ::: "memory"); }
__device__ __forceinline__ void lds_set(int off, unsigned v) { asm volatile("ds_write_b32 %0, %1" ::"v"(off), "v"(v) : "memory"); }
__device__ __forceinline__ void lds_wait(LAS char* L, int word, unsigned want) {
  volatile LAS unsigned* f = (volatile LAS unsigned*)(L + L_FLAG) + word;
#pragma unroll 1
  for (unsigned n = 0; n < (SPIN_CAP << 4); ++n) {
    if ((unsigned)__builtin_amdgcn_readfirstlane(*f) >= want) break;
    __builtin_amdgcn_s_sleep(1);
  }
  asm volatile("" ::: "memory");
}
__device__ __forceinline__ v4u ld128(rsrc_t rs, int off) { return __builtin_amdgcn_raw_buffer_load_b128(rs, off, 0, AUX_SC1); }
__device__ __forceinline__ v2u ld64(rsrc_t rs, int off) { return __builtin_amdgcn_raw_buffer_load_b64(rs, off, 0, AUX_SC1); }
__device__ __forceinline__ unsigned ld32(rsrc_t rs, int off) { return __builtin_amdgcn_raw_buffer_load_b32(rs, off, 0, AUX_SC1); }
__device__ __forceinline__ void st128(v4u v, rsrc_t rs, int off) { __builtin_amdgcn_raw_buffer_store_b128(v, rs, off, 0, AUX_SC1); }
__device__ __forceinline__ void st64(v2u v, rsrc_t rs, int off) { __builtin_amdgcn_raw_buffer_store_b64(v, rs, off, 0, AUX_SC1); }
__device__ __forceinline__ void st32(unsigned v, rsrc_t rs, int off) { __builtin_amdgcn_raw_buffer_store_b32(v, rs, off, 0, AUX_SC1); }
constexpr int OOB = 0x40000000;
__device__ __forceinline__ v4u ldsr128(LAS char* L, int off) { return *(LAS v4u*)(L + off); }
__device__ __forceinline__ v4u trfrag(LAS char* L, int off, int half_stride) {
  v4s a = __builtin_amdgcn_ds_read_tr16_b64_v4i16((LAS v4s*)(L + off));
  v4s b = __builtin_amdgcn_ds_read_tr16_b64_v4i16((LAS v4s*)(L + off + half_stride));
  v2u x = __builtin_bit_cast(v2u, a), y = __builtin_bit_cast(v2u, b);
  return v4u{x[0], x[1], y[0], y[1]};
}
__device__ __forceinline__ void acc8(float (&a)[8], v4u v) {
#pragma unroll
  for (int i = 0; i < 4; ++i) { a[2 * i] += bflo(v[i]); a[2 * i + 1] += bfhi(v[i]); }
}
#define ST(k)
#define STL(k)
#define STDUMP()

template <int W, int O0, int O1, int BAR_AT, int FLAG_AT, int FLAG>
__device__ __forceinline__ void stream(rsrc_t rs, LAS char* dst, const int (&vo)[4], int s0, int sa, int sb) {
#pragma unroll 1
  for (int o = O0; o < O1; ++o) {
    if (BAR_AT >= 0 && o == BAR_AT) bar();
    waitvm<W - 4>();
    if (FLAG_AT >= 0 && o == FLAG_AT) lds_set(L_FLAG + FLAG * 4, 1u);
    const int so = s0 + (o >> 1) * sa + (o & 1) * sb;
#pragma unroll
    for (int u = 0; u < 4; ++u)
      __builtin_amdgcn_raw_ptr_buffer_load_lds(rs, (LAS void*)(dst + (o * 4 + u) * 1024), 16, vo[u], so, 0, AUX_NT);
  }
}
__device__ __forceinline__ rsrc_t mkrs(const void* p, int bytes) { return __builtin_amdgcn_make_buffer_rsrc((void*)p, 0, bytes, 0x00020000); }
__device__ __forceinline__ void vo_w256(int (&vo)[4], int lane, int pitch) {
  const int r = lane >> 4, s = lane & 15;
#pragma unroll
  for (int u = 0; u < 4; ++u) vo[u] = (4 * u + r) * pitch + ((((s >> 1) ^ ((4 * u + r) & 7)) << 5) | ((s & 1) << 4));
}
__device__ __forceinline__ void vo_w512(int (&vo)[4], int lane, int pitch) {
  const int r = lane >> 5, s = lane & 31;
#pragma unroll
  for (int u = 0; u < 4; ++u) vo[u] = (2 * u + r) * pitch + (((s >> 4) << 8) | ((((s >> 1) & 7) ^ ((2 * u + r) & 7)) << 5) | ((s & 1) << 4));
}
__device__ __forceinline__ void vo_k(int (&vo)[4], int lane) {
  const int r = lane >> 4, s = lane & 15;
#pragma unroll
  for (int u = 0; u < 4; ++u) vo[u] = (4 * u + r) * 256 + ((s ^ (4 * u + r)) << 4);
}
__device__ __forceinline__ int tr_base(int lane, int pitch, int cb) {
  const int row = lane >> 2;
  return row * pitch + ((cb >> 3) << 8) + (((cb & 7) ^ (row & 7)) << 5) + ((lane & 3) << 3);
}

__device__ __forceinline__ v4f sel4(bool c, v4f a, v4f b) { return v4f{c ? a[0] : b[0], c ? a[1] : b[1], c ? a[2] : b[2], c ? a[3] : b[3]}; }

template <bool isX, int KIND>
__device__ __forceinline__ void role(const Params& p, LAS char* L, const int lane, const int wave) {
  const int xcd = blockIdx.x & 7, jj = blockIdx.x >> 3;
  const int ci = jj >> 1;
  const int cid = xcd * 16 + ci;
  const int id5 = xcd * 14 + ci;

  if constexpr (KIND == 0) {
    int vo[4];
    STL(0);
    if (isX) {
      const int kv0 = ((xcd * 4 + (ci >> 2)) * 4096 + 3072 + (ci & 3) * 256) * 256;
      vo_k(vo, lane);
      stream<W_K, 0, 16, BAR0_AT, -1, 0>(mkrs(p.kc, 8 * 4 * 4096 * 256), L + L_RA, vo, kv0, 8192, 4096);
      STL(1);
      vo_w256(vo, lane, 256);
      stream<W_V, 0, 16, -1, (W_V - 4 + 3) / 4, F_T0>(mkrs(p.vc, 8 * 4 * 4096 * 256), L + L_RB, vo, kv0, 8192, 4096);
      STL(2);
      bar();
      waitvm<0>();
      STL(3);
      bar();
      if (ci < 14) {
        vo_w512(vo, lane, 7168);
        stream<W_1, 0, 16, -1, -1, 0>(mkrs(p.w1, 1024 * 7168), L + L_RA, vo, xcd * 128 * 7168 + ci * 512, 16 * 7168, 8 * 7168);
        STL(4);
        waitvm<0>();
        lds_set(L_FLAG + F_T2 * 4, 1u);
        STL(5);
      }
      bar();
    } else {
      vo_w256(vo, lane, 8192);
      stream<W_Q, 0, 16, BAR0_AT, -1, 0>(mkrs(p.wq, 1024 * 8192), L + L_RA, vo, (cid & 3) * 256 * 8192 + (cid >> 2) * 256, 32 * 8192, 16 * 8192);
      STL(1);
      vo_w256(vo, lane, 2048);
      const int rs3 = cid >> 3;
      const int wo0 = ((rs3 >> 2) * 1024 + (rs3 & 3) * 32) * 2048 + (cid & 7) * 256;
      const rsrc_t rwo = mkrs(p.wo, 4096 * 2048);
      stream<W_OA, 0, N_OA, -1, (W_OA - 4 + 3) / 4, F_T0>(rwo, L + L_RB, vo, wo0, 128 * 2048, 16 * 2048);
      STL(2);
      stream<W_OB, N_OA, 16, -1, -1, 0>(rwo, L + L_RB, vo, wo0, 128 * 2048, 16 * 2048);
      waitvm<0>();
      lds_set(L_FLAG + F_T1 * 4, 1u);
      STL(3);
      bar();
      if (ci < 14) {
        vo_w512(vo, lane, 2048);
        stream<W_2, 0, 16, -1, -1, 0>(mkrs(p.w2, 3584 * 2048), L + L_RA, vo, (id5 >> 2) * 128 * 2048 + (id5 & 3) * 512, 16 * 2048, 8 * 2048);
        STL(4);
        waitvm<0>();
        lds_set(L_FLAG + F_T2 * 4, 1u);
        STL(5);
        bar();
      }
    }
    STDUMP();
    return;
  }

  const rsrc_t rsw = mkrs(p.ws, WS_END);
  const v2u ep = ld64(rsw, WS_E);
  const int b16 = lane & 15, g4 = lane >> 4, hw = lane >> 5;
  const int fr_off = g4 * 128 + (lane & 7) * 16;

  if constexpr (KIND == 1) {
    const int mw = wave - 1;
    const v4u sv = {SENT, SENT, SENT, SENT};
    if (isX) {
      if (lane < 8) *(LAS v4u*)(L + L_HIMG + mw * 640 + 512 + lane * 16) = v4u{0x3F803F80u, 0x3F803F80u, 0x3F803F80u, 0x3F803F80u};
      const int g = ci >> 2, s = ci & 3;
      int a0 = WS_O + (((g * 4 + mw) * 8 + xcd) * 4 + s) * 512 + lane * 16;
      if (lane >= 32) a0 = (lane < 40 && mw < 2) ? (mw == 0 ? WS_LSE + ((g * 8 + xcd) * 4 + s) * 128 : WS_STAT + (ci * 8 + xcd) * 128) + (lane - 32) * 16 : OOB;
      st128(sv, rsw, a0);
      const int a1 = (lane < 40 && ci < 14) ? WS_P4 + ((ci * 8 + 2 * mw) * 8 + xcd) * 640 + lane * 16 : OOB;
      st128(sv, rsw, a1);
      st128(sv, rsw, a1 + 8 * 640);
    } else {
      st128(sv, rsw, WS_Q + (((2 * mw + hw) * 32 + (cid >> 2)) * 4 + (cid & 3)) * 512 + (lane & 31) * 16);
      st128(sv, rsw, lane < 32 ? WS_P3 + (((cid & 7) * 4 + mw) * 16 + (cid >> 3)) * 512 + lane * 16 : OOB);
      st128(sv, rsw, ci < 14 ? WS_P5 + ((((id5 & 3) * 8 + 2 * mw + hw) * 4 + ((lane >> 3) & 3)) * 28 + (id5 >> 2)) * 128 + (lane & 7) * 16 : OOB);
    }
    waitvm<0>();
    {
      unsigned old = 0;
      if (lane == 0) old = __hip_atomic_fetch_add((LAS unsigned*)(L + L_FLAG) + F_INIT, 1u, __ATOMIC_RELAXED, __HIP_MEMORY_SCOPE_WORKGROUP);
      old = __builtin_amdgcn_readfirstlane(old);
      if (old == 3u) {
        const u64 tag = (((u64)ep[1] << 32) | ep[0]) + 1ull;
        st64(v2u{(unsigned)tag, (unsigned)(tag >> 32)}, rsw, lane == 0 ? (isX ? WS_RDYX : WS_RDYY) + cid * 8 : OOB);
      }
    }
#pragma unroll 1
    for (int it0 = DRYRUN ? 0 : 1; it0 < 2; ++it0) {
      int it = it0;
      asm volatile("" : "+s"(it));
      const bool dry = it == 0;
      const rsrc_t rs = mkrs(p.ws, dry ? 0 : WS_END);
      if (isX) {
        if (!dry) { bar(); bar(); }
        if (!dry && ci >= 14) { bar(); break; }
        if (!dry) lds_wait(L, F_T2, 1u);
        ST(0);
        v4u a[4][4];
#pragma unroll
        for (int c = 0; c < 4; ++c) {
          const int tb = L_RA + tr_base(lane, 512, 4 * mw + c);
#pragma unroll
          for (int t = 0; t < 4; ++t) a[c][t] = trfrag(L, tb + t * 32 * 512, 16 * 512);
        }
        ST(1);
        if (!dry) bar();
        ST(2);
        const int ho = L_HIMG + (b16 < 8 ? fr_off : 512);
        v4f acc[4] = {};
#pragma unroll
        for (int t = 0; t < 4; ++t) {
          const v4u bf = ldsr128(L, ho + t * 640);
#pragma unroll
          for (int c = 0; c < 4; ++c) acc[c] = mfma(a[c][t], bf, acc[c]);
        }
        const int po = b16 < 8 ? b16 * 64 + g4 * 16 : (b16 < 10 ? 512 + (b16 - 8) * 64 + g4 * 16 : OOB);
#pragma unroll
        for (int f = 0; f < 2; ++f) {
          const v4f c0 = acc[2 * f], c1 = acc[2 * f + 1], cs = sel4(b16 == 8, c0, c1);
          v4u o = {pk(c0[0], c0[1]), pk(c0[2], c0[3]), pk(c1[0], c1[1]), pk(c1[2], c1[3])};
          if (b16 >= 8) o = __builtin_bit_cast(v4u, cs);
          o = v4u{nosent(o[0]), nosent(o[1]), nosent(o[2]), nosent(o[3])};
          st128(o, rs, WS_P4 + ((ci * 8 + 2 * mw + f) * 8 + xcd) * 640 + po);
        }
        ST(3);
      } else {
#pragma unroll 1
        for (int r = 0; r < 2; ++r) {
          if (dry && r != (mw & 1)) continue;
          if (dry && mw >= 2) continue;
          if (!dry) lds_wait(L, r == 0 ? F_T0 : F_T1, 1u);
          ST(r * 4 + 0);
          v4u a[2][8];
#pragma unroll
          for (int c = 0; c < 2; ++c) {
            const int tb = (r == 0 ? L_RA : L_RB) + tr_base(lane, 256, 2 * mw + c);
#pragma unroll
            for (int t = 0; t < 8; ++t) a[c][t] = trfrag(L, tb + t * 32 * 256, 16 * 256);
          }
          ST(r * 4 + 1);
          if (!dry) {
            if (r == 0) { lds_wait(L, F_LN, 8u); lds_wait(L, F_INIT, 4u); }
            else bar();
          }
          ST(r * 4 + 2);
          const int bo = (r == 0 ? L_YIMG : L_OIMG) + fr_off;
          v4f acc[2] = {};
#pragma unroll
          for (int t = 0; t < 8; ++t) {
            const v4u bf = ldsr128(L, bo + t * 512);
#pragma unroll
            for (int c = 0; c < 2; ++c) acc[c] = mfma(a[c][t], bf, acc[c]);
          }
          v4u o; int off;
          if (r == 0) {
            o = __builtin_bit_cast(v4u, sel4(b16 < 8, acc[0], acc[1]));
            off = WS_Q + (((lane & 7) * 32 + (cid >> 2)) * 4 + (cid & 3)) * 512 + mw * 128 + (b16 >> 3) * 64 + g4 * 16;
          } else {
            o = v4u{pk(acc[0][0], acc[0][1]), pk(acc[0][2], acc[0][3]), pk(acc[1][0], acc[1][1]), pk(acc[1][2], acc[1][3])};
            off = b16 < 8 ? WS_P3 + (((cid & 7) * 4 + mw) * 16 + (cid >> 3)) * 512 + b16 * 64 + g4 * 16 : OOB;
          }
          o = v4u{nosent(o[0]), nosent(o[1]), nosent(o[2]), nosent(o[3])};
          st128(o, rs, off);
          ST(r * 4 + 3);
        }
        if (!dry && ci >= 14) break;
        if (dry && mw < 2) continue;
        if (!dry) lds_wait(L, F_T2, 1u);
        ST(8);
        v4u a[4][4];
#pragma unroll
        for (int c = 0; c < 4; ++c) {
          const int tb = L_RA + tr_base(lane, 512, 4 * mw + c);
#pragma unroll
          for (int t = 0; t < 4; ++t) a[c][t] = trfrag(L, tb + t * 32 * 512, 16 * 512);
        }
        ST(9);
        if (!dry) bar();
        ST(10);
        v4f acc[4] = {};
#pragma unroll
        for (int t = 0; t < 4; ++t) {
          const v4u bf = ldsr128(L, L_AIMG + fr_off + t * 512);
#pragma unroll
          for (int c = 0; c < 4; ++c) acc[c] = mfma(a[c][t], bf, acc[c]);
        }
        const v4f c0 = sel4(b16 < 8, acc[0], acc[2]), c1 = sel4(b16 < 8, acc[1], acc[3]);
        v4u o = {nosent(pk(c0[0], c0[1])), nosent(pk(c0[2], c0[3])), nosent(pk(c1[0], c1[1])), nosent(pk(c1[2], c1[3]))};
        st128(o, rs, WS_P5 + ((((id5 & 3) * 8 + 2 * mw + (b16 >> 3)) * 4 + ((lane & 7) >> 1)) * 28 + (id5 >> 2)) * 128 + (lane & 1) * 64 + g4 * 16);
        ST(11);
      }
    }
    STDUMP();
    return;
  }

  const int gw = wave - 5;
  const int gf = gw & 3, gbh = gw >> 2;
  const int gb = 4 * gbh + ((lane >> 2) & 3);
  const int gk = 16 * hw + 4 * (lane & 3) + 2 * (g4 & 1);
  const int gimg = (lane & 3) * 128 + gb * 16 + hw * 8 + (g4 & 1) * 4;
  const int gpo = gb * 64 + (lane & 3) * 16;
  unsigned xpair = 0;
  if (isX) {
    xpair = *(const GAS unsigned*)(p.x + gb * 1024 + xcd * 128 + gf * 32 + gk);
  } else {
    const GAS u16* xr = (const GAS u16*)p.x + gw * 1024;
    const v4u x0 = *(const GAS v4u*)(xr + lane * 8), x1 = *(const GAS v4u*)(xr + 512 + lane * 8);
    const v2u xq = *(const GAS v2u*)(xr + (cid & 3) * 256 + lane * 4);
    float s = 0.f;
#pragma unroll
    for (int i = 0; i < 4; ++i) s += (bflo(x0[i]) + bfhi(x0[i])) + (bflo(x1[i]) + bfhi(x1[i]));
    const float mean = wave_sum(s) * (1.f / 1024.f);
    float q = 0.f;
#pragma unroll
    for (int i = 0; i < 4; ++i) {
      const float d0 = bflo(x0[i]) - mean, d1 = bfhi(x0[i]) - mean, d2 = bflo(x1[i]) - mean, d3 = bfhi(x1[i]) - mean;
      q += (d0 * d0 + d1 * d1) + (d2 * d2 + d3 * d3);
    }
    const float var = rbf(wave_sum(q) * (1.f / 1024.f)), m = rbf(mean);
    const float den = rbf(sqrtf(rbf(var + 1e-5f)));
    const v2u y = {pk(rbf(bflo(xq[0]) - m) / den, rbf(bfhi(xq[0]) - m) / den), pk(rbf(bflo(xq[1]) - m) / den, rbf(bfhi(xq[1]) - m) / den)};
    *(LAS v2u*)(L + L_YIMG + (lane >> 3) * 512 + (lane & 3) * 128 + gw * 16 + ((lane >> 2) & 1) * 8) = y;
    asm volatile("s_waitcnt lgkmcnt(0)" ::: "memory");
    if (lane == 0) __hip_atomic_fetch_add((LAS unsigned*)(L + L_FLAG) + F_LN, 1u, __ATOMIC_RELAXED, __HIP_MEMORY_SCOPE_WORKGROUP);
  }
  const u64 tag = (((u64)ep[1] << 32) | ep[0]) + 1ull;
#pragma unroll 1
  for (int it0 = DRYRUN ? 0 : 1; it0 < 2; ++it0) {
    int it = it0;
    asm volatile("" : "+s"(it));
    const bool dry = it == 0;
    const rsrc_t rs = mkrs(p.ws, dry ? 0 : WS_END);
    if (!dry) {
      ST(0);
      _Pragma("unroll 1") for (unsigned n = 0; n < SPIN_CAP; ++n) {
        const v4u r = ld128(rs, (isX ? WS_RDYY : WS_RDYX) + lane * 16);
        const bool ok = r[0] == (unsigned)tag && r[1] == (unsigned)(tag >> 32) && r[2] == (unsigned)tag && r[3] == (unsigned)(tag >> 32);
        if (__all(ok)) break;
        __builtin_amdgcn_s_sleep(8);
      }
      __builtin_amdgcn_fence(__ATOMIC_ACQUIRE, "wavefront");
      ST(1);
    }
    if (isX) {
      const int g = ci >> 2, sp = ci & 3;
      if (!dry || (gw & 3) == 0) {
        if (!dry) lds_wait(L, F_T0, 1u);
        v4u kf[2][4];
#pragma unroll
        for (int t = 0; t < 4; ++t) {
          const int ko = L_RA + (32 * gw + b16) * 256 + (((4 * t + g4) ^ b16) << 4);
          kf[0][t] = ldsr128(L, ko);
          kf[1][t] = ldsr128(L, ko + 4096);
        }
        ST(2);
        const int qo = WS_Q + ((xcd * 32 + g * 8 + gw) * 4) * 512 + lane * 16;
        v4u q0, q1;
        _Pragma("unroll 1") for (unsigned n = 0; n < SPIN_CAP; ++n) {
          q0 = ld128(rs, qo); q1 = ld128(rs, qo + 1024);
          if (__all(umax4(q0, umax4(q1, 0u)) != SENT)) break;
          __builtin_amdgcn_s_sleep(POLL_SLEEP);
        }
        ST(3);
        const v4f qs = __builtin_bit_cast(v4f, q0) + __builtin_bit_cast(v4f, q1);
        const int d = 4 * (lane & 31) + 2 * hw;
        *(LAS unsigned*)(L + L_QIMG + (d >> 5) * 512 + ((d >> 3) & 3) * 128 + gw * 16 + (d & 7) * 2) = pk(rs32(qs[0], qs[2]), rs32(qs[1], qs[3]));
        if (!dry) bar();
        ST(4);
        v4f sc[2] = {};
#pragma unroll
        for (int t = 0; t < 4; ++t) {
          const v4u qf = ldsr128(L, L_QIMG + fr_off + t * 512);
          sc[0] = mfma(kf[0][t], qf, sc[0]);
          sc[1] = mfma(kf[1][t], qf, sc[1]);
        }
        float s[8];
#pragma unroll
        for (int i = 0; i < 4; ++i) {
          const unsigned r1 = pk(sc[i >> 1][2 * (i & 1)], sc[i >> 1][2 * (i & 1) + 1]);
          const unsigned r2 = pk(bflo(r1) * 0.08838834764831845f, bfhi(r1) * 0.08838834764831845f);
          s[2 * i] = bflo(r2); s[2 * i + 1] = bfhi(r2);
        }
        float m = fmaxf(fmaxf(fmaxf(s[0], s[1]), fmaxf(s[2], s[3])), fmaxf(fmaxf(s[4], s[5]), fmaxf(s[6], s[7])));
        m = mx32(mx16(m));
        float l = 0.f;
#pragma unroll
        for (int i = 0; i < 8; ++i) { s[i] = __builtin_amdgcn_exp2f((s[i] - m) * 1.4426950408889634f); l += s[i]; }
        l = rs16(l, l); l = rs32(l, l);
        if (b16 < 8) {
          *(LAS v4u*)(L + L_PIMG + gw * 512 + fr_off) = v4u{pk(s[0], s[1]), pk(s[2], s[3]), pk(s[4], s[5]), pk(s[6], s[7])};
          if (g4 == 0) *(LAS v2f*)(L + L_ML + (b16 * 8 + gw) * 8) = v2f{m * 1.4426950408889634f, l};
        }
        ST(5);
        if (!dry) bar();
        ST(6);
      }
      if (!dry || (gw & 3) == 1) {
        const int tb = L_RB + tr_base(lane, 256, gw);
        v4f d[8];
        v4f ml[4];
#pragma unroll
        for (int t = 0; t < 4; ++t) ml[t] = *(LAS v4f*)(L + L_ML + (lane & 7) * 64 + t * 16);
#pragma unroll
        for (int t = 0; t < 8; ++t) d[t] = mfma(trfrag(L, tb + t * 32 * 256, 16 * 256), ldsr128(L, L_PIMG + fr_off + t * 512), v4f{0.f, 0.f, 0.f, 0.f});
        float M = fmaxf(fmaxf(fmaxf(ml[0][0], ml[0][2]), fmaxf(ml[1][0], ml[1][2])), fmaxf(fmaxf(ml[2][0], ml[2][2]), fmaxf(ml[3][0], ml[3][2])));
        float f[8], Ls = 0.f;
#pragma unroll
        for (int t = 0; t < 8; ++t) { f[t] = __builtin_amdgcn_exp2f(ml[t >> 1][2 * (t & 1)] - M); Ls += f[t] * ml[t >> 1][2 * (t & 1) + 1]; }
        if (gw == 0 || dry) st32(nosent(__float_as_uint(M + __builtin_amdgcn_logf(Ls))), rs, lane < 8 ? WS_LSE + ((g * 8 + xcd) * 4 + sp) * 128 + lane * 4 : OOB);
        const float inv = __builtin_amdgcn_rcpf(Ls);
        v4f o = d[0] * f[0];
#pragma unroll
        for (int t = 1; t < 8; ++t) o += d[t] * f[t];
        o *= inv;
        st64(v2u{nosent(pk(o[0], o[1])), nosent(pk(o[2], o[3]))}, rs,
             b16 < 8 ? WS_O + (((g * 4 + (gw >> 1)) * 8 + xcd) * 4 + sp) * 512 + (gw & 1) * 256 + b16 * 32 + g4 * 8 : OOB);
        ST(7);
      }
      if (!dry || (gw & 3) == 2) {
        const int po = WS_P3 + ((xcd * 4 + gf) * 16 + g4) * 512 + gpo;
        v4u v[4];
        _Pragma("unroll 1") for (unsigned n = 0; n < SPIN_CAP; ++n) {
#pragma unroll
          for (int i = 0; i < 4; ++i) v[i] = ld128(rs, po + i * 2048);
          if (__all(umax4(v[0], umax4(v[1], umax4(v[2], umax4(v[3], 0u)))) != SENT)) break;
          __builtin_amdgcn_s_sleep(POLL_SLEEP);
        }
        ST(8);
        float a[8] = {};
#pragma unroll
        for (int i = 0; i < 4; ++i) acc8(a, v[i]);
        const float r0 = rs32(a[0], a[4]), r1 = rs32(a[1], a[5]), r2 = rs32(a[2], a[6]), r3 = rs32(a[3], a[7]);
        const unsigned at = pk(rs16(r0, r2), rs16(r1, r3));
        *(LAS unsigned*)(L + L_HIMG + gf * 640 + gimg) = pk(bflo(xpair) + bflo(at), bfhi(xpair) + bfhi(at));
        if (!dry) bar();
        ST(9);
      }
      if (dry ? gw == 3 : gw == 0) {
        const int ho = L_HIMG + g4 * 640 + ((lane >> 3) & 1) * 256 + (lane & 7) * 16;
        const v4u h0 = ldsr128(L, ho), h1 = ldsr128(L, ho + 128);
        float s = 0.f, q = 0.f;
#pragma unroll
        for (int i = 0; i < 4; ++i) {
          const float e0 = bflo(h0[i]), e1 = bfhi(h0[i]), e2 = bflo(h1[i]), e3 = bfhi(h1[i]);
          s += (e0 + e1) + (e2 + e3);
          q += (e0 * e0 + e1 * e1) + (e2 * e2 + e3 * e3);
        }
        s += dpp<ROR8>(s); q += dpp<ROR8>(q);
        s = rs16(s, s); q = rs16(q, q);
        s = rs32(s, s); q = rs32(q, q);
        st64(v2u{nosent(__float_as_uint(s)), nosent(__float_as_uint(q))}, rs, (lane < 8 && ci < 14) ? WS_STAT + (ci * 8 + xcd) * 128 + lane * 8 : OOB);
        ST(10);
      }
      if (dry ? gw == 7 : gw == 1) {
        const int f = ci >> 2, jb = ci & 3;
        const int ob = 2 * jb + ((lane >> 2) & 1), og = lane & 3, oj = 4 * hw + 2 * (g4 & 1) + ((lane >> 3) & 1);
        const float hv = bflo(*(LAS u16*)(L + L_HIMG + f * 640 + og * 128 + ob * 16 + oj * 2));
        const int po = WS_P5 + (((xcd * 4 + f) * 4 + jb) * 28) * 128 + lane * 16;
        v4u v[4];
        _Pragma("unroll 1") for (unsigned n = 0; n < SPIN_CAP; ++n) {
#pragma unroll
          for (int i = 0; i < 3; ++i) v[i] = ld128(rs, po + i * 1024);
          v[3] = ld128(rs, lane < 32 ? po + 3072 : OOB);
          if (__all(umax4(v[0], umax4(v[1], umax4(v[2], umax4(v[3], 0u)))) != SENT)) break;
          __builtin_amdgcn_s_sleep(POLL_SLEEP);
        }
        ST(11);
        float a[8] = {};
#pragma unroll
        for (int i = 0; i < 4; ++i) acc8(a, v[i]);
        const float r0 = rs32(a[0], a[4]), r1 = rs32(a[1], a[5]), r2 = rs32(a[2], a[6]), r3 = rs32(a[3], a[7]);
        float u0 = rs16(r0, r2), u1 = rs16(r1, r3);
        u0 += dpp<ROR8>(u0); u1 += dpp<ROR8>(u1);
        const float ff = rbf((lane & 8) ? u1 : u0);
        const rsrc_t ro = mkrs(p.out, dry ? 0 : 16384);
        __builtin_amdgcn_raw_buffer_store_b16((u16)pk(hv + ff, 0.f), ro, (ob * 1024 + xcd * 128 + f * 32 + 16 * (oj >> 2) + 4 * og + (oj & 3)) * 2, 0, 0);
        if (cid == 0) st64(v2u{(unsigned)tag, (unsigned)(tag >> 32)}, rs, lane == 0 ? WS_E : OOB);
        ST(12);
      }
    } else {
      if (!dry || (gw & 1) == 0) {
        const int rs3 = cid >> 3;
        const int oo = WS_O + ((rs3 * 8 + gw) * 4) * 512 + lane * 16;
        const int lo = WS_LSE + (((rs3 >> 2) * 8 + gw) * 4 + hw) * 128 + ((lane >> 1) & 7) * 4;
        v4u v0, v1; unsigned e0, e1;
        _Pragma("unroll 1") for (unsigned n = 0; n < SPIN_CAP; ++n) {
          e0 = ld32(rs, lo); e1 = ld32(rs, lo + 256);
          v0 = ld128(rs, oo); v1 = ld128(rs, oo + 1024);
          if (__all(umax4(v0, umax4(v1, e0 > e1 ? e0 : e1)) != SENT)) break;
          __builtin_amdgcn_s_sleep(POLL_SLEEP);
        }
        ST(2);
        const float l0 = __uint_as_float(e0), l1 = __uint_as_float(e1);
        const float M = mx32(fmaxf(l0, l1));
        float w0 = __builtin_amdgcn_exp2f(l0 - M), w1 = __builtin_amdgcn_exp2f(l1 - M);
        const float ws_ = w0 + w1;
        const float inv = __builtin_amdgcn_rcpf(rs32(ws_, ws_));
        w0 *= inv; w1 *= inv;
        float a[8];
#pragma unroll
        for (int i = 0; i < 4; ++i) { a[2 * i] = w0 * bflo(v0[i]) + w1 * bflo(v1[i]); a[2 * i + 1] = w0 * bfhi(v0[i]) + w1 * bfhi(v1[i]); }
        const v2u o = {pk(rs32(a[0], a[4]), rs32(a[1], a[5])), pk(rs32(a[2], a[6]), rs32(a[3], a[7]))};
        *(LAS v2u*)(L + L_OIMG + ((lane >> 1) & 7) * 512 + (2 * (lane & 1) + hw) * 128 + gw * 16 + (g4 & 1) * 8) = o;
        if (!dry) bar();
        ST(3);
      }
      if (!dry && ci >= 14) break;
      if (!dry || (gw & 1) == 1) {
        const int F = (id5 >> 2) * 4 + gf;
        const int po = WS_P4 + (F * 8 + g4) * 640 + gpo;
        const int co = WS_P4 + (F * 8 + 2 * ((lane >> 2) & 3)) * 640 + 512 + gk * 4;
        const int so = WS_STAT + ((id5 >> 3) * 8 + (lane & 3) + 4 * (g4 & 1)) * 128 + gb * 8;
        v4u v0, v1; v2u c0, c1, sq;
        _Pragma("unroll 1") for (unsigned n = 0; n < SPIN_CAP; ++n) {
          sq = ld64(rs, so); c0 = ld64(rs, co); c1 = ld64(rs, co + 640);
          v0 = ld128(rs, po); v1 = ld128(rs, po + 4 * 640);
          if (__all(umax4(v0, umax4(v1, umax3(umax3(sq[0], sq[1], c0[0]), umax3(c0[1], c1[0], c1[1]), 0u))) != SENT)) break;
          __builtin_amdgcn_s_sleep(POLL_SLEEP);
        }
        ST(4);
        float s = __uint_as_float(sq[0]), q = __uint_as_float(sq[1]);
        s += dpp<XOR1>(s); q += dpp<XOR1>(q); s += dpp<XOR2>(s); q += dpp<XOR2>(q);
        s = rs16(s, s); q = rs16(q, q);
        const float mean = s * (1.f / 1024.f);
        const float var = rbf(fmaxf(q * (1.f / 1024.f) - mean * mean, 0.f)), m = rbf(mean);
        const float inv = __builtin_amdgcn_rcpf(rbf(__builtin_amdgcn_sqrtf(rbf(var + 1e-5f))));
        float ca = __uint_as_float(c0[0]) + __uint_as_float(c1[0]), cb = __uint_as_float(c0[1]) + __uint_as_float(c1[1]);
        ca += dpp<ROR8>(ca); cb += dpp<ROR8>(cb); ca += dpp<ROR4>(ca); cb += dpp<ROR4>(cb);
        float a[8] = {};
        acc8(a, v0); acc8(a, v1);
        const float r0 = rs32(a[0], a[4]), r1 = rs32(a[1], a[5]), r2 = rs32(a[2], a[6]), r3 = rs32(a[3], a[7]);
        const float u0 = (rs16(r0, r2) - m * ca) * inv, u1 = (rs16(r1, r3) - m * cb) * inv;
        *(LAS unsigned*)(L + L_AIMG + gf * 512 + gimg) = pk(u0 < 0.f ? 0.f : u0, u1 < 0.f ? 0.f : u1);
        if (!dry) bar();
        ST(5);
      }
    }
  }
  STDUMP();
}

__global__ void __launch_bounds__(NTHR) fused(Params p) {
  extern __shared__ __attribute__((aligned(16))) char smem[];
  LAS char* L = (LAS char*)smem;
  const int lane = threadIdx.x & 63, wave = __builtin_amdgcn_readfirstlane(threadIdx.x >> 6);
  const bool isX = (blockIdx.x & 8) == 0;
  if (__builtin_expect(wave == 0, 1)) {
    const u64 t0 = __builtin_amdgcn_s_memrealtime();
    if (isX) role<true, 0>(p, L, lane, wave); else role<false, 0>(p, L, lane, wave);
    while (__builtin_amdgcn_s_memrealtime() - t0 < 4000) __builtin_amdgcn_s_sleep(1);
    __builtin_amdgcn_endpgm();
  }
  if (wave == 1) lds_set(L_FLAG + (lane & 15) * 4, 0u);
  bar();
  if (wave <= 4) {
    if (isX) { role<true, 1>(p, L, lane, wave); __builtin_amdgcn_endpgm(); }
    role<false, 1>(p, L, lane, wave); __builtin_amdgcn_endpgm();
  }
  if (isX) { role<true, 2>(p, L, lane, wave); __builtin_amdgcn_endpgm(); }
  role<false, 2>(p, L, lane, wave);
}
}

extern "C" void kernel_launch(void* const* d_in, const int* in_sizes, int n_in, void* d_out, int out_size, void* d_ws, size_t ws_size,
                              hipStream_t stream) {
  mk::Params p;
  p.x = (const mk::u16*)d_in[0]; p.kc = (const mk::u16*)d_in[1]; p.vc = (const mk::u16*)d_in[2];
  p.wq = (const mk::u16*)d_in[3]; p.wo = (const mk::u16*)d_in[4]; p.w1 = (const mk::u16*)d_in[6]; p.w2 = (const mk::u16*)d_in[7];
  p.out = (mk::u16*)d_out; p.ws = (char*)d_ws;
  (void)hipFuncSetAttribute((const void*)mk::fused, hipFuncAttributeMaxDynamicSharedMemorySize, mk::L_END);
  mk::fused<<<256, mk::NTHR, mk::L_END, stream>>>(p);
}
```

```cpp
#define CODE_BYTES 15840
#include <hip/hip_runtime.h>
#include <stdint.h>

#define WS_AT 0
#define WS_OFF(sz) (WS_AT == 0 ? (size_t)0 : (WS_AT == 1 ? (((sz) - (8u << 20)) & ~(size_t)0x1FFFFF) : (size_t)(WS_AT) << 20))
#define ROLE_OF(c) (((c) & 8) != 0)
namespace mk {
typedef unsigned short u16;
typedef unsigned long long u64;
typedef unsigned v4u __attribute__((ext_vector_type(4)));
typedef unsigned v2u __attribute__((ext_vector_type(2)));
typedef float v4f __attribute__((ext_vector_type(4)));
typedef float v2f __attribute__((ext_vector_type(2)));
typedef short v4s __attribute__((ext_vector_type(4)));
typedef __bf16 v8bf __attribute__((ext_vector_type(8)));
typedef __bf16 v2bf __attribute__((ext_vector_type(2)));
#define LAS __attribute__((address_space(3)))
#define GAS __attribute__((address_space(1)))
typedef __amdgpu_buffer_rsrc_t rsrc_t;

#define N_Y0 8
#define W_F 11
#define N_K0 2
#define N_KA 8
#define W_KA 8
#define P_KA 24
#define P_K 2
#define K_F 8
#define T_O 430
#define T_SLOW 60
#define P_O 6
#define N_E1 4
#define N_E2 6
#define T_E1 560
#define T_E2 590
#define P_1 2
#define P_2 0
#define T_S 40
#define RDY_A 70
#define RDY_B 240
#define H0 2
#define ST_SLEEP 28
#define S_Y 0
#define S_XC 20
#define P4LATE 0
#define QDUAL 0
#define YM 2
#define YM_SLEEP 40
#define RDY_C 0
#define XF_AT 50
#define N_PROBE 2
#define FORCE_FLIP -1
#define NEAR 1
#define XROT 0
#define VTRY 0
#define PV_AT 2
#define S1_AT 5
#define WAIT_ST 3
#define N_XP 0
#define N_WPX 0
#define N_WPY 0
#define T_WE 130
#define HD0 0
#define HD1 0
#define HD2 0
#define HD3 3
#define HD4 0
#define HPRE 0
#define TREF 2
#define TREF2_OFF 95
#define XF 2
#define DRYRUN 0

constexpr int NWAVE = 16, NTHR = NWAVE * 64;
constexpr unsigned SENT = 0xFFFFFFFFu;
constexpr int AUX_SC1 = 16;
constexpr unsigned SPIN_CAP = 1u << 17;
constexpr int WS_E = 0, WS_FLIP = 1024, WS_PROBE = 98304, WS_RDY = 4096  , WS_HINT = 16384  , WS_Q = 131072;
constexpr int WS_O = WS_Q + 524288, WS_ML = WS_O + 262144, WS_P3 = WS_ML + 16384, WS_STAT = WS_P3 + 262144, WS_P5 = WS_STAT + 16384,
              WS_P4 = 2 << 20, WS_Q2 = 3 << 20, WS_H02 = 114688  , WS_END = QDUAL ? 4 << 20 : WS_P4 + 112 * 8192;
static_assert(WS_P5 + 128 * 4096 <= WS_P4, "ws map");
__device__ __forceinline__ int ws_at(int base, int side, int half, int o) {
  return NEAR ? base + (o & 8191) + ((o >> 13) << 14) + (((side ^ (NEAR - 1) ^ (base >> 21)) & 1) << 13) : base + side * half + o;
}
constexpr int BLK = NEAR ? 16384 : 8192;
__device__ __forceinline__ int q_at(int b, int h) { return QDUAL ? ws_at(WS_Q2, b >> 2, 0, (b * 32 + h) * 2048) : WS_Q + (b * 32 + h) * 2048; }
__device__ __forceinline__ int o_at(int rs3, int b) { return ws_at(WS_O, rs3 >> 3, 131072, ((rs3 & 7) * 8 + b) * 2048); }
__device__ __forceinline__ int ml_at(int g, int b) { return ws_at(WS_ML, g >> 1, 8192, ((g & 1) * 8 + b) * 512); }
__device__ __forceinline__ int p3_at(int cb, int f) { return ws_at(WS_P3, cb >> 2, 131072, ((cb & 3) * 4 + f) * 8192); }
__device__ __forceinline__ int p4_at(int F) { return ws_at(WS_P4, F >= 56, 56 * 8192, (F >= 56 ? F - 56 : F) * 8192); }
__device__ __forceinline__ int st_at(int pn) { return ws_at(WS_STAT, pn < 14 ? pn >= 7 : pn & 1, 8192, (pn < 14 ? pn % 7 : 7) * 1024); }
__device__ __forceinline__ int p5_at(int F5) { return ws_at(WS_P5, F5 >> 4, 262144, (F5 & 15) * 16384); }
__device__ __forceinline__ int p5_jb(int jb) { return (jb >> 1) * BLK + (jb & 1) * 4096; }
constexpr int P4F = BLK, P5F = 2 * BLK;
constexpr int WS_STAMP = 6 << 20;
constexpr int L_RA = 0, L_RB = 65536, L_M = 131072;
constexpr int L_QIMG = L_M, L_PIMG = L_M + 4096, L_HIMG = L_PIMG + 4096;
constexpr int L_YIMG = L_M, L_OIMG = L_YIMG + 4096, L_AIMG = L_OIMG + 4096;
constexpr int L_FLAG = L_M + 11264, L_MX = L_FLAG + 64, L_STAMP = L_FLAG + 128, L_JUNK = L_STAMP + NWAVE * 16 * 4, L_X = L_JUNK + 512, L_END = L_X + 16384;
static_assert(L_END <= 163840 && L_X % 16 == 0 && L_PIMG == L_OIMG, "LDS");
constexpr int F_QR = 0, F_Q = 1, F_T1 = 2, F_T2 = 4, F_H = 6, F_RDY = 7, F_K = 8, F_INIT = 9, F_ST = 10, F_P = 11;

struct Params {
  const u16 *wq, *x;
  char* ws;
  const u16 *kc, *vc, *wo, *w1, *w2;
  u16* out;
};

__device__ __forceinline__ int sel(bool c, int a, int b) { return c ? a : b; }
__device__ __forceinline__ float self(bool c, float a, float b) { return c ? a : b; }
__device__ __forceinline__ float bflo(unsigned u) { return __uint_as_float(u << 16); }
__device__ __forceinline__ float bfhi(unsigned u) { return __uint_as_float(u & 0xffff0000u); }
__device__ __forceinline__ unsigned pk(float a, float b) { v2f f = {a, b}; return __builtin_bit_cast(unsigned, __builtin_convertvector(f, v2bf)); }
__device__ __forceinline__ v4u pk8(v4f a, v4f b) { return v4u{pk(a[0], a[1]), pk(a[2], a[3]), pk(b[0], b[1]), pk(b[2], b[3])}; }
__device__ __forceinline__ unsigned relu2(unsigned u) {
  typedef short v2s __attribute__((ext_vector_type(2)));
  const v2s a = __builtin_bit_cast(v2s, u), lim = {(short)0xFF80, (short)0xFF80};
  const v2s keep = __builtin_elementwise_sub_sat(lim, a) >> 15;
  return u & __builtin_bit_cast(unsigned, keep);
}
__device__ __forceinline__ float rbf(float a) { return bflo(pk(a, a)); }
__device__ __forceinline__ float dot2(unsigned a, unsigned b, float c) {
  return __builtin_amdgcn_fdot2_f32_bf16(__builtin_bit_cast(v2bf, a), __builtin_bit_cast(v2bf, b), c, false);
}
__device__ __forceinline__ float max3(float a, float b, float c) { float r; asm("v_max3_f32 %0, %1, %2, %3" : "=v"(r) : "v"(a), "v"(b), "v"(c)); return r; }
__device__ __forceinline__ unsigned umax3(unsigned a, unsigned b, unsigned c) { unsigned m = a > b ? a : b; return m > c ? m : c; }
__device__ __forceinline__ unsigned umax4(v4u v, unsigned m) { return umax3(umax3(v[0], v[1], m), v[2], v[3]); }
__device__ __forceinline__ v4f mfma(v4u a, v4u b, v4f c) {
  return __builtin_amdgcn_mfma_f32_16x16x32_bf16(__builtin_bit_cast(v8bf, a), __builtin_bit_cast(v8bf, b), c, 0, 0, 0);
}
__device__ __forceinline__ float rs32(float a, float b) {
  auto s = __builtin_amdgcn_permlane32_swap(__float_as_uint(a), __float_as_uint(b), false, false);
  return __uint_as_float(s[0]) + __uint_as_float(s[1]);
}
__device__ __forceinline__ float rs16(float a, float b) {
  auto s = __builtin_amdgcn_permlane16_swap(__float_as_uint(a), __float_as_uint(b), false, false);
  return __uint_as_float(s[0]) + __uint_as_float(s[1]);
}
template <int CTRL> __device__ __forceinline__ float dpp(float x) {
  return __builtin_bit_cast(float, __builtin_amdgcn_mov_dpp(__builtin_bit_cast(int, x), CTRL, 0xf, 0xf, true));
}
constexpr int XOR1 = 0xB1, XOR2 = 0x4E, ROR4 = 0x124, ROR8 = 0x128;
__device__ __forceinline__ v4f sel4(bool c, v4f a, v4f b) { return v4f{c ? a[0] : b[0], c ? a[1] : b[1], c ? a[2] : b[2], c ? a[3] : b[3]}; }
__device__ __forceinline__ float ln_inv(float mean, float meansq) {
  const float vr = meansq - mean * mean;
  return __builtin_amdgcn_rcpf(rbf(__builtin_amdgcn_sqrtf(rbf(rbf(vr < 0.f ? 0.f : vr) + 1e-5f))));
}
__device__ __forceinline__ unsigned now() { return (unsigned)__builtin_amdgcn_s_memrealtime(); }
__device__ __forceinline__ void wait_until(unsigned t) { while ((int)(now() - t) < 0) __builtin_amdgcn_s_sleep(1); }
template <int N> __device__ __forceinline__ void waitvm() { asm volatile("s_waitcnt vmcnt(%0)" ::"i"(N) : "memory"); }
__device__ __forceinline__ void bar() { asm volatile("s_waitcnt lgkmcnt(0)\n\ts_barrier" ::: "memory"); }
__device__ __forceinline__ void bar_if(int live) { if (__builtin_constant_p(live)) bar(); else asm volatile("s_cmp_eq_u32 %0, 0\n\ts_cbranch_scc1 1f\n\ts_waitcnt lgkmcnt(0)\n\ts_barrier\n1:" ::"s"(live) : "memory", "scc"); }
__device__ __forceinline__ void barr_if(int live) { if (__builtin_constant_p(live)) asm volatile("s_barrier" ::: "memory"); else asm volatile("s_cmp_eq_u32 %0, 0\n\ts_cbranch_scc1 1f\n\ts_barrier\n1:" ::"s"(live) : "memory", "scc"); }
__device__ __forceinline__ void lds_set(int off, unsigned v) { asm volatile("ds_write_b32 %0, %1" ::"v"(off), "v"(v) : "memory"); }
__device__ __forceinline__ unsigned lds_wait(int word, unsigned want) {
  unsigned n, v;
  asm volatile(
      "s_mov_b32 %0, 0x1000000\n"
      "1:\n\tds_read_b32 %1, %2\n\ts_sub_u32 %0, %0, 1\n\ts_waitcnt lgkmcnt(0)\n\ts_cbranch_scc1 2f\n\t"
      "v_cmp_gt_u32 vcc, %3, %1\n\ts_cbranch_vccnz 1b\n"
      "2:"
      : "=&s"(n), "=&v"(v) : "v"(L_FLAG + word * 4), "s"(want) : "memory", "scc", "vcc");
  return v;
}
__device__ __forceinline__ void lds_wait7(int word, int gw) {
  unsigned n, v;
  asm volatile(
      "s_cmp_lg_u32 %3, 7\n\ts_cbranch_scc1 2f\n\ts_mov_b32 %0, 0x1000000\n"
      "1:\n\tds_read_b32 %1, %2\n\ts_sub_u32 %0, %0, 1\n\ts_waitcnt lgkmcnt(0)\n\ts_cbranch_scc1 2f\n\t"
      "v_cmp_gt_u32 vcc, 4, %1\n\ts_cbranch_vccnz 1b\n"
      "2:"
      : "=&s"(n), "=&v"(v) : "v"(L_FLAG + word * 4), "s"(gw) : "memory", "scc", "vcc");
}
__device__ __forceinline__ void lds_inc(int a) { asm volatile("ds_add_u32 %0, %1" ::"v"(a), "v"(1u) : "memory"); }
__device__ __forceinline__ int inc_addr(int lane, int word) { return sel(lane == 0, L_FLAG, L_JUNK + lane * 4) + word * 4; }
__device__ __forceinline__ v4u ld128(rsrc_t rs, int off, int so) { return __builtin_amdgcn_raw_buffer_load_b128(rs, off, so, AUX_SC1); }
__device__ __forceinline__ v2u ld64(rsrc_t rs, int off, int so) { return __builtin_amdgcn_raw_buffer_load_b64(rs, off, so, AUX_SC1); }
__device__ __forceinline__ unsigned ld32(rsrc_t rs, int off, int so) { return __builtin_amdgcn_raw_buffer_load_b32(rs, off, so, AUX_SC1); }
__device__ __forceinline__ void st128(v4u v, rsrc_t rs, int off, int so) { __builtin_amdgcn_raw_buffer_store_b128(v, rs, off, so, AUX_SC1); }
__device__ __forceinline__ void st64(v2u v, rsrc_t rs, int off, int so) { __builtin_amdgcn_raw_buffer_store_b64(v, rs, off, so, AUX_SC1); }
__device__ __forceinline__ void st32(unsigned v, rsrc_t rs, int off, int so) { __builtin_amdgcn_raw_buffer_store_b32(v, rs, off, so, AUX_SC1); }
constexpr int OOB = 0x40000000;
__device__ __forceinline__ v4u ldsr128(LAS char* L, int off) { return *(LAS v4u*)(L + off); }
__device__ __forceinline__ v4u trfrag(LAS char* L, int off, int half_stride) {
  v4s a = __builtin_amdgcn_ds_read_tr16_b64_v4i16((LAS v4s*)(L + off));
  v4s b = __builtin_amdgcn_ds_read_tr16_b64_v4i16((LAS v4s*)(L + off + half_stride));
  v2u x = __builtin_bit_cast(v2u, a), y = __builtin_bit_cast(v2u, b);
  return v4u{x[0], x[1], y[0], y[1]};
}
__device__ __forceinline__ v4u mksrd(const void* p, unsigned bytes) { const u64 a = (u64)p; return v4u{(unsigned)a, (unsigned)(a >> 32) & 0xFFFFu, bytes, 0x00020000u}; }
__device__ __forceinline__ rsrc_t mkrs(const void* p, int bytes) { return __builtin_amdgcn_make_buffer_rsrc((void*)p, 0, bytes, 0x00020000); }
__device__ __forceinline__ int hint_at(int hop, int cid) { return WS_HINT + (hop * 128 + (NEAR == 2 ? cid ^ 64 : cid)) * 128; }
__device__ __forceinline__ void hint_send(v4u srd, int off) { asm volatile("buffer_atomic_add %0, %1, %2, 0 offen sc1" ::"v"(1u), "v"(off), "s"(srd) : "memory"); }
template <int NB, int CAP>
__device__ __forceinline__ void hint_poll(const char* ws, int live, int hop, int cid, unsigned step) {
  unsigned n = live * CAP; int h;
  asm volatile(
      "1:\n\ts_sub_u32 %0, %0, 1\n\ts_cbranch_scc1 2f\n\ts_load_dword %1, %2, 0x0 glc\n\ts_waitcnt lgkmcnt(0)\n\ts_cmp_lt_i32 %1, %3\n\ts_cbranch_scc1 1b\n2:"
      : "+s"(n), "=&s"(h) : "s"(ws + hint_at(hop, cid)), "i"(NB - 1) : "memory", "scc");
  if (step) lds_set(L_JUNK + live * (L_FLAG + F_H * 4 - L_JUNK), step);
}
__device__ __forceinline__ v4u sel_sum(int lane) {
  const int i = lane & 15;
  const unsigned one = (i & 2) ? 0u : ((i & 1) ? 0x3F800000u : 0x3F80u);
  return v4u{(i >> 2) == 0 ? one : 0u, (i >> 2) == 1 ? one : 0u, (i >> 2) == 2 ? one : 0u, (i >> 2) == 3 ? one : 0u};
}
__device__ __forceinline__ v4u sel_split(int lane, int h) {
  const int i = lane & 15;
  const unsigned one = 2 * h + ((i >> 1) & 1) == (lane >> 4) ? ((i & 1) ? 0x3F800000u : 0x3F80u) : 0u;
  return v4u{(i >> 2) == 0 ? one : 0u, (i >> 2) == 1 ? one : 0u, (i >> 2) == 2 ? one : 0u, (i >> 2) == 3 ? one : 0u};
}
__device__ __forceinline__ u64 any_nan(float a, float b) { return __builtin_amdgcn_ballot_w64(__builtin_isunordered(a, b)); }
#define LIKELY(x) __builtin_expect(!!(x), 1)
#define UNLIKELY(x) __builtin_expect(!!(x), 0)
#define FAKE(x) asm volatile("" : "=v"(x))
#define KEEP(x) asm volatile("" ::"v"(x))
#define PIN(x) asm volatile("" : "+v"(x))
#define FRESH(l) int l = lane0; asm volatile("" : "+v"(l))
constexpr int live = 1;
#define TWICE(bit) constexpr bool dry_ = (DRYRUN & (bit)) != 0; _Pragma("unroll 1") for (int it0_ = dry_ ? 0 : 1; it0_ < 2; ++it0_)
#define LIVE(prio) int live = dry_ ? it0_ : 1; if (dry_) asm volatile("s_setprio 0\n\ts_cmp_eq_u32 %0, 0\n\ts_cbranch_scc1 1f\n\ts_setprio " #prio "\n1:" : "+s"(live)::"scc"); else __builtin_amdgcn_s_setprio(prio)
#define HINT_PRE(hop, flag) if (HPRE >> hop & 1) lds_wait7(flag, gw)
template <bool PRE>
__device__ __forceinline__ void hint_bar(v4u srd, int off, unsigned one, int snd) {
  if (PRE) asm volatile("s_cmp_lg_u32 %3, 7\n\ts_cbranch_scc1 1f\n\tbuffer_atomic_add %0, %1, %2, 0 offen sc1\n1:\n\ts_waitcnt lgkmcnt(0)\n\ts_barrier" ::"v"(one), "v"(off), "s"(srd), "s"(snd) : "memory", "scc");
  else asm volatile("s_cmp_lg_u32 %3, 7\n\ts_waitcnt lgkmcnt(0)\n\ts_barrier\n\ts_cbranch_scc1 1f\n\tbuffer_atomic_add %0, %1, %2, 0 offen sc1\n1:" ::"v"(one), "v"(off), "s"(srd), "s"(snd) : "memory", "scc");
}
#define HINT_BAR(hop, off) hint_bar<(HPRE >> hop & 1) != 0>(rw, off, one, gw)
template <int NB, int CAP, int POLLER, int DELAY>
__device__ __forceinline__ void hint_wait(const char* a, int gw, unsigned step, int fh) {
  unsigned n, v; int h;
  asm volatile(
      "s_cmp_lg_u32 %4, %9\n\ts_cbranch_scc1 3f\n\ts_mov_b32 %0, %7\n"
      "1:\n\ts_sub_u32 %0, %0, 1\n\ts_cbranch_scc1 2f\n\ts_load_dword %1, %3, 0x0 glc\n\ts_waitcnt lgkmcnt(0)\n\ts_cmp_lt_i32 %1, %8\n\ts_cbranch_scc1 1b\n"
      "2:\n\t.if %10\n\ts_sleep %10\n\t.endif\n\tds_write_b32 %6, %5\n\ts_sleep 1\n\ts_branch 5f\n"
      "3:\n\ts_mov_b32 %0, 0x1000000\n"
      "4:\n\tds_read_b32 %2, %6\n\ts_sub_u32 %0, %0, 1\n\ts_waitcnt lgkmcnt(0)\n\ts_cbranch_scc1 5f\n\tv_cmp_gt_u32 vcc, %5, %2\n\ts_cbranch_vccnz 4b\n"
      "5:"
      : "=&s"(n), "=&s"(h), "=&v"(v) : "s"(a), "s"(gw), "v"(step), "v"(fh), "i"(CAP), "i"(NB - 1), "i"(POLLER), "i"(DELAY) : "memory", "scc", "vcc");
}
#define COLD_BEGIN ".subsection 1\n1:\n\t"
#define COLD_END "v_cmp_eq_u32 vcc, -1, %2\n\ts_sub_u32 %1, %1, 1\n\ts_cselect_b64 vcc, 0, vcc\n\ts_cselect_b32 %1, 0, %1\n\ts_or_b32 %0, vcc_lo, vcc_hi\n\ts_sleep 1\n\ts_branch 2f\n\t.subsection 0\n2:"
__device__ __forceinline__ unsigned again(unsigned& n, float a, float b, v4u x, v4u y, v4u z, v4u w) {
  unsigned r, t;
  asm volatile("v_cmp_u_f32 vcc, %3, %4\n\ts_mov_b32 %0, 0\n\ts_cbranch_vccnz 1f\n\t" COLD_BEGIN
               "v_max3_u32 %2, %5, %6, %7\n\tv_max3_u32 %2, %2, %8, %9\n\tv_max3_u32 %2, %2, %10, %11\n\tv_max3_u32 %2, %2, %12, %13\n\t"
               "v_max3_u32 %2, %2, %14, %15\n\tv_max3_u32 %2, %2, %16, %17\n\tv_max3_u32 %2, %2, %18, %19\n\tv_max_u32 %2, %2, %20\n\t" COLD_END
               : "=&s"(r), "+s"(n), "=&v"(t)
               : "v"(a), "v"(b), "v"(x[0]), "v"(x[1]), "v"(x[2]), "v"(x[3]), "v"(y[0]), "v"(y[1]), "v"(y[2]), "v"(y[3]),
                 "v"(z[0]), "v"(z[1]), "v"(z[2]), "v"(z[3]), "v"(w[0]), "v"(w[1]), "v"(w[2]), "v"(w[3])
               : "memory", "vcc", "scc");
  return r;
}
__device__ __forceinline__ unsigned again_q(unsigned& n, unsigned d) {
  unsigned r, t;
  asm volatile("v_cmp_ne_u32 vcc, 0, %3\n\ts_mov_b32 %0, 0\n\ts_cbranch_vccnz 1f\n\t" COLD_BEGIN "v_mov_b32 %2, -1\n\t" COLD_END
               : "=&s"(r), "+s"(n), "=&v"(t) : "v"(d) : "memory", "vcc", "scc");
  return r;
}
#define PINS(x) asm volatile("" : "+s"(x))
#define ST(k)
#define STB(k)
#define STN(k)
#define STV(k, v)
#define STX(k)
#define STDUMP()

__device__ __forceinline__ unsigned bidx() { return (blockIdx.x & ~7u) | ((blockIdx.x + XROT) & 7u); }
__device__ __forceinline__ void dma1(v4u srd, int dst, int vo, int so) {
  asm volatile("s_mov_b32 m0, %3\n\ts_nop 0\n\tbuffer_load_dwordx4 %0, %1, %2 offen sc0 nt sc1 lds" ::"v"(vo), "s"(srd), "s"(so), "s"(dst) : "memory");
}
template <int W, int PACE, bool QUAD = false, int FLAG_AT = -1>
__device__ __forceinline__ void stream(const int O0, const int O1, v4u srd, int dst, int vo, int s0, int sa, int sb, int flag = 0) {
#pragma unroll 1
  for (int o = O0; o < O1; ++o) {
    const int u = QUAD ? (o >> 2) + (o & 3) * 4 : o;
    const int so = s0 + (u >> 1) * sa + (u & 1) * sb;
    waitvm<W - 1>();
    dma1(srd, dst + u * 4096, vo, so);
    if (PACE >= 4) __builtin_amdgcn_s_sleep(PACE / 4);
    if (PACE & 3) asm volatile("s_nop %0" ::"i"(4 * (PACE & 3) - 1));
    if (FLAG_AT >= 0 && o == FLAG_AT) { waitvm<FLAG_AT + 1>(); lds_inc(flag); }
  }
}
__device__ __forceinline__ int vo_w256(int lane, int u, int pitch) {
  const int r = 4 * u + (lane >> 4);
  return r * pitch + (((lane & 15) ^ ((r & 7) << 1)) << 4);
}
__device__ __forceinline__ int vo_w512(int lane, int u, int pitch) {
  const int r = 2 * u + (lane >> 5);
  return r * pitch + (((lane & 31) ^ (r << 1)) << 4);
}
__device__ __forceinline__ int vo_k(int lane, int u) {
  const int r = 4 * u + (lane >> 4);
  return r * 256 + (((lane ^ r) & 15) << 4);
}
__device__ __forceinline__ int kv_base() {
  const int ci = bidx() >> 4;
  return (((bidx() & 7) * 4 + (ci >> 2)) * 4096 + 3072 + (ci & 3) * 256) * 256;
}
__device__ __forceinline__ int tr_base(int lane, int pitch, int cb) {
  const int row = lane >> 2;
  return row * pitch + ((cb ^ (row & 7)) << 5) + ((lane & 3) << 3);
}
__device__ __forceinline__ int fr_off(int lane) { return (lane >> 4) * 128 + (lane & 7) * 16; }
__device__ __forceinline__ u64 get_tag(const char* ws) {
  u64 e;
  asm volatile("s_load_dwordx2 %0, %1, 0x0\n\ts_waitcnt lgkmcnt(0)" : "=s"(e) : "s"(ws + WS_E) : "memory");
  return e + 0x0101010101010101ull;
}
__device__ __forceinline__ unsigned qtag(u64 tag, int piece) { return ((piece & 1) ? (unsigned)(tag >> 32) ^ 0x9C5A3F61u : (unsigned)tag ^ 0x2DE78B14u); }
__device__ __forceinline__ void rdy_check(const int who, const rsrc_t rs, const u64 tag, const int tell = L_FLAG + F_RDY * 4) {
#pragma unroll 1
  for (unsigned n = 0; n < SPIN_CAP; ++n) {
    const v2u r = ld64(rs, who * 32, WS_RDY);
    if (__builtin_amdgcn_ballot_w64(((r[0] ^ (unsigned)tag) | (r[1] ^ (unsigned)(tag >> 32))) != 0u) == 0) break;
    __builtin_amdgcn_s_sleep(12);
  }
  lds_set(tell, 1u);
}

__device__ __forceinline__ void flip_decide(const char* ws, const rsrc_t rs, int lane) {
  unsigned best[2] = {~0u, ~0u}, j0, j1;
  u64 tag;
  asm volatile("s_load_dwordx2 %0, %3, 0x0\n\ts_load_dword %1, %3, %4 glc\n\ts_load_dword %2, %3, %5 glc\n\ts_waitcnt lgkmcnt(0)"
               : "=&s"(tag), "=&s"(j0), "=&s"(j1) : "s"(ws + WS_E), "i"(WS_PROBE), "i"(WS_PROBE + 8192) : "memory");
  tag += 0x0101010101010101ull;
#pragma unroll 1
  for (int i = 0; i < 2 * N_PROBE; ++i) {
    u64 t0, t1; unsigned v;
    asm volatile("s_memtime %0\n\ts_waitcnt lgkmcnt(0)\n\ts_load_dword %2, %3, 0x0 glc\n\ts_waitcnt lgkmcnt(0)\n\ts_memtime %1\n\ts_waitcnt lgkmcnt(0)"
                 : "=&s"(t0), "=&s"(t1), "=&s"(v) : "s"(ws + WS_PROBE + (i & 1) * 8192) : "memory");
    const unsigned d = (unsigned)t1 - (unsigned)t0, b = best[0];
    best[0] = best[1]; best[1] = d < b ? d : b;
  }
  const unsigned f = FORCE_FLIP >= 0 ? FORCE_FLIP : (best[1] < best[0] ? 1u : 0u);
  st64(v2u{((unsigned)tag & ~1u) | f, (unsigned)(tag >> 32)}, rs, sel(lane == 0, 0, OOB), WS_FLIP);
}
__device__ __forceinline__ int flip_read(const int lane, const int who, const rsrc_t rs, const u64 tag) {
  unsigned f = 0;
#pragma unroll 1
  for (unsigned n = 0; n < SPIN_CAP; ++n) {
    const v2u r = ld64(rs, sel(lane == 0, WS_FLIP, WS_RDY + who * 32), 0);
    f = __builtin_amdgcn_readfirstlane(r[0]) & 1u;
    if (__builtin_amdgcn_ballot_w64((((r[0] ^ (unsigned)tag) & sel(lane == 0, ~1, ~0)) | (r[1] ^ (unsigned)(tag >> 32))) != 0u) == 0) break;
    __builtin_amdgcn_s_sleep(8);
  }
  lds_set(L_FLAG + F_P * 4, 1u + (f << 13));
  return f << 13;
}
__device__ __forceinline__ int flip_wait() { return __builtin_amdgcn_readfirstlane(lds_wait(F_P, 1u)) & 8192; }
__device__ __forceinline__ int pmir(int pn) { return pn < 7 ? pn + 7 : (pn < 14 ? pn - 7 : pn ^ 1); }

template <bool isX>
__device__ __forceinline__ void loader(const Params& p, LAS char* L, const int lane0, const int wave, const unsigned t_in) {
  const int xcd = bidx() & 7, ci = bidx() >> 4, cid = xcd * 16 + ci, id5 = xcd * 14 + ci;
  const int ra = L_RA + wave * 1024, rb = L_RB + wave * 1024;
  const int lane = lane0;
  const int fa = inc_addr(lane, 0);
  constexpr int pitch = isX ? 7168 : 2048;
  const v4u r3 = mksrd(isX ? p.w1 : p.w2, ci < 14 ? 1024 * 7168 : 0);
  const int s3 = isX ? xcd * 128 * 7168 + ci * 512 : (id5 >> 2) * 128 * 2048 + (id5 & 3) * 512;
  int n3;
  if (isX) {
    const int kv0 = kv_base();
    const v4u rk = mksrd(p.kc, 8 * 4 * 4096 * 256);
    const int vk = vo_k(lane, wave);
    if (XF != 2) stream<W_KA, P_KA, true>(N_K0, N_KA, rk, ra, vk, kv0, 8192, 4096);
    stream<W_F, P_K, true>(N_KA, 16, rk, ra, vk, kv0, 8192, 4096);
    STX(6);
    stream<W_F, P_K, true, K_F>(0, 16, mksrd(p.vc, 8 * 4 * 4096 * 256), rb, vo_w256(lane, wave, 256), kv0, 8192, 4096, fa + F_K * 4);
    STX(2);
    const v4u rw = mksrd(p.ws, wave == 0 ? WS_END : 0);
    int h1 = sel(lane < 32, hint_at(1, (ci >> 2) * 32 + lane), OOB);
    const int vo3 = vo_w512(lane, wave, pitch);
    waitvm<8>();
    lds_inc(fa + F_T1 * 4);
    bar();
    unsigned fw;
    asm volatile("ds_read_b32 %0, %1" : "=v"(fw) : "v"(L_FLAG + F_P * 4) : "memory");
    bool vin = false;
    if (VTRY) {
      const unsigned ib = __builtin_amdgcn_s_getreg(7 | (31 << 11));
      vin = ((ib & 15u) | (ib >> 30)) == 0u;
      if (vin) { waitvm<0>(); lds_inc(fa + (F_T1 + 1) * 4); }
    }
    stream<64, 0>(0, N_E1 / 2, r3, ra, vo3, s3, 16 * pitch, 8 * pitch);
    bar();
    asm volatile("" : "+v"(fw));
    h1 ^= fw & 8192; PIN(h1);
    stream<64, 0>(N_E1 / 2, N_E1, r3, ra, vo3, s3, 16 * pitch, 8 * pitch);
    bar();
    if (!vin) { waitvm<N_E1>(); lds_inc(fa + (F_T1 + 1) * 4); }
    hint_send(rw, h1);
    STX(3);
    n3 = N_E1;
  } else {
    STX(9);
    const int rs3 = cid >> 3;
    const int wo0 = ((rs3 >> 2) * 1024 + (rs3 & 3) * 32) * 2048 + (cid & 7) * 256;
    lds_wait(F_QR, 4u);
    wait_until(t_in + T_O - ((cid & 3) == 1 ? T_SLOW : 0));
    stream<W_F, P_O>(0, 16, mksrd(p.wo, 4096 * 2048), rb, vo_w256(lane, wave, 2048), wo0, 128 * 2048, 16 * 2048);
    STX(2);
    const int late = (int)(now() - t_in);
    n3 = late < T_E1 ? N_E2 : (late < T_E2 ? N_E2 / 2 : 0);
    stream<W_F, P_O>(0, n3, r3, ra, vo_w512(lane, wave, pitch), s3, 16 * pitch, 8 * pitch);
    if (n3 == N_E2) waitvm<N_E2 + 4>(); else if (n3) waitvm<N_E2 / 2 + 4>(); else waitvm<4>();
    lds_inc(fa + F_T1 * 4);
    if (n3 == N_E2) waitvm<N_E2>(); else if (n3) waitvm<N_E2 / 2>(); else waitvm<0>();
    lds_inc(fa + (F_T1 + 1) * 4);
    STX(3);
    bar();
  }
  if (ci < 14) {
    if (WAIT_ST & (isX ? 1 : 2)) lds_wait(F_ST, 4u);
    stream<W_F, (isX ? P_1 : P_2)>(n3, 16, r3, ra, vo_w512(lane, wave, pitch), s3, 16 * pitch, 8 * pitch);
    STX(4);
    constexpr int NX = isX ? N_XP : 0;
    if (NX) stream<W_F, P_1>(16 - NX, 16, mksrd(p.w2, 1024 * 7168), rb, vo_w512(lane, wave, 2048), (id5 >> 2) * 128 * 2048 + (id5 & 3) * 512, 16 * 2048, 8 * 2048);
    waitvm<4 + NX>();
    lds_inc(fa + F_T2 * 4);
    waitvm<NX>();
    lds_inc(fa + (F_T2 + 1) * 4);
    STX(5);
  } else if (isX ? N_WPX : N_WPY) {
    const v4u rp = mksrd(p.w2, 1024 * 7168);
    const int vp = vo_w512(lane, wave, 2048);
    lds_wait(F_ST, 4u);
    const unsigned t0 = now();
    constexpr int hi = isX ? 16 - N_WPY : 16, lo = hi - (isX ? N_WPX : N_WPY);
#pragma unroll 1
    for (int j = 0; j < 7; ++j) {
      const int t5 = xcd * 14 + (ci - 14) * 7 + j;
      stream<W_F, 0>(lo, hi, rp, ra, vp, (t5 >> 2) * 128 * 2048 + (t5 & 3) * 512, 16 * 2048, 8 * 2048);
      if ((int)(now() - t0) > T_WE) break;
    }
  }
  STDUMP();
}

template <bool isX>
__device__ __forceinline__ void mrole(const Params& p, LAS char* L, const int lane0, const int wave, const rsrc_t rs, const unsigned t_in) {
  const int xcd = bidx() & 7, ci = bidx() >> 4, cid = xcd * 16 + ci, id5 = xcd * 14 + ci;
  const int mw = wave - 4;
  int ps2 = isX ? o_at((ci >> 2) * 4 + mw, xcd) + (ci & 3) * 512 : p3_at(cid & 7, mw) + (cid >> 3) * 512;
  int ps3 = isX ? p4_at(ci * 8 + 2 * mw) + xcd * 640 : p5_at((id5 & 3) * 8 + 2 * mw) + (id5 >> 2) * 128;
  const u64 tag = get_tag(p.ws);
  if (!isX && YM == 1) { const int fl = flip_wait(); ps2 ^= fl; ps3 ^= fl; }
  {
    FRESH(lane);
    const v4u sv = {SENT, SENT, SENT, SENT};
    if (isX) {
      *(LAS v4u*)(L + L_HIMG + mw * 640 + 512 + (lane & 7) * 16) = v4u{0x3F803F80u, 0x3F803F80u, 0x3F803F80u, 0x3F803F80u};
      wait_until(t_in + T_S);
      const int ax = mw == 0 ? ml_at(ci >> 2, xcd) + (ci & 3) * 128 : (mw == 1 ? st_at(ci) + xcd * 128 : OOB);
      const int a0 = sel(lane < 32, ps2, sel(lane < 40, ax - 512, OOB)) + lane * 16;
      st128(sv, rs, a0, 0);
      const int a1 = sel(lane < (ci < 14 ? 40 : 0), lane * 16, OOB);
      st128(sv, rs, a1, ps3); st128(sv, rs, a1, ps3 + P4F);
    } else {
      if (S_Y) __builtin_amdgcn_s_sleep(S_Y);
      st128(sv, rs, sel(lane < 32, lane * 16, OOB), ps2);
      st128(sv, rs, (lane >> 5) * P5F + p5_jb((lane >> 3) & 3) + (lane & 7) * 16 + (ci < 14 ? 0 : OOB), ps3);
    }
    waitvm<0>();
    unsigned old;
    asm volatile("ds_add_rtn_u32 %0, %1, %2\n\ts_waitcnt lgkmcnt(0)" : "=v"(old) : "v"(inc_addr(lane, F_INIT)), "v"(1u) : "memory");
    st64(v2u{(unsigned)tag, (unsigned)(tag >> 32)}, rs, sel((lane == 0) & (old == 3u), 0, OOB), WS_RDY + ((isX ? 0 : 128) + cid) * 32);
    STX(14);
  }
  if (isX && mw == 3) { if (XF_AT) __builtin_amdgcn_s_sleep(XF_AT); else lds_wait(F_H, 1u); flip_read(lane0, xcd * 16 + sel(lane0 & 1, ci ^ 8, pmir(ci)), rs, tag); }
  if (!isX) {
    if (YM != 1) { const int fl = flip_wait(); ps2 ^= fl; ps3 ^= fl; }
    PINS(ps2); PINS(ps3);
    if (YM == 2 && mw == 3) {
      const unsigned j5 = ci < 14 ? id5 ^ 2 : 0;
      __builtin_amdgcn_s_sleep(YM_SLEEP);
      rdy_check(128 + sel(lane0 & 1, cid ^ 4, j5 + (j5 / 14) * 2), rs, tag, L_JUNK);
    }
  }
  TWICE(isX ? 4 : 32) {
    LIVE(3);
    const rsrc_t rs = mkrs(p.ws, WS_END * live);
    {
      FRESH(lane);
      int po = sel(lane & 8, OOB, (lane & 7) * 64 + (lane >> 4) * 16), fo = L_OIMG + fr_off(lane), sa = live ? inc_addr(lane, F_ST) : L_JUNK;
      const int tb0 = L_RB + tr_base(lane, 256, 2 * mw), tb1 = L_RB + tr_base(lane, 256, 2 * mw + 1);
      PIN(po); PIN(fo); PIN(sa);
      constexpr int E = isX ? 4 : 6;
#define KS(t) (isX ? (((t) & 3) * 2 + ((t) >> 2)) : (t))
      lds_wait(F_T1, 4u * live);
      ST(4);
      v4u a[2][8];
#pragma unroll
      for (int t = 0; t < E; ++t) { a[0][t] = trfrag(L, tb0 + KS(t) * 32 * 256, 16 * 256); a[1][t] = trfrag(L, tb1 + KS(t) * 32 * 256, 16 * 256); }
      bool vin = false;
      if (isX) {
        barr_if(live);
        const unsigned fw = *(LAS unsigned*)(L + L_FLAG + F_P * 4);
        barr_if(live);
        { const int fl = __builtin_amdgcn_readfirstlane(fw) & 8192; ps2 ^= fl; ps3 ^= fl; PINS(ps2); PINS(ps3); }
        if (VTRY) {
          unsigned h;
          asm volatile("ds_read_b32 %0, %1\n\ts_waitcnt lgkmcnt(0)" : "=v"(h) : "v"(L_FLAG + (F_T1 + 1) * 4) : "memory");
          vin = __builtin_amdgcn_readfirstlane(h) >= 4;
          if (vin) {
#pragma unroll
            for (int u = E; u < 8; ++u) { a[0][u] = trfrag(L, tb0 + KS(u) * 32 * 256, 16 * 256); a[1][u] = trfrag(L, tb1 + KS(u) * 32 * 256, 16 * 256); }
          } else {
#pragma unroll
            for (int u = E; u < 8; ++u) { FAKE(a[0][u]); FAKE(a[1][u]); }
          }
        }
      } else {
        lds_wait(F_T1 + 1, 4u * live);
#pragma unroll
        for (int t = E; t < 8; ++t) { a[0][t] = trfrag(L, tb0 + t * 32 * 256, 16 * 256); a[1][t] = trfrag(L, tb1 + t * 32 * 256, 16 * 256); }
      }
      ST(5);
      barr_if(live);
      ST(6);
      v4u bf[8];
#pragma unroll
      for (int t = 0; t < 8; ++t) { bf[t] = ldsr128(L, fo + KS(t) * 512); if (isX && t == 3) __builtin_amdgcn_sched_barrier(0); }
      __builtin_amdgcn_sched_barrier(0);
      v4f acc[2] = {};
#pragma unroll
      for (int t = 0; t < 8; ++t) {
        if (isX && t == PV_AT) {
          PIN(acc[0]); PIN(acc[1]);
          if (!vin) {
            lds_wait(F_T1 + 1, 4u * live);
#pragma unroll
            for (int u = E; u < 8; ++u) { a[0][u] = trfrag(L, tb0 + KS(u) * 32 * 256, 16 * 256); a[1][u] = trfrag(L, tb1 + KS(u) * 32 * 256, 16 * 256); }
          }
          __builtin_amdgcn_sched_barrier(0);
        }
        acc[0] = mfma(a[0][t], bf[t], acc[0]);
        acc[1] = mfma(a[1][t], bf[t], acc[1]);
      }
      __builtin_amdgcn_sched_barrier(0);
      const v4u o = pk8(acc[0], acc[1]);
      st128(o, rs, po, ps2);
      lds_inc(sa);
      KEEP(o);
      ST(7);
    }
    if (ci < 14) {
      FRESH(lane);
      const int b16 = lane & 15, g4 = lane >> 4;
      int bo = isX ? L_HIMG + sel(b16 < 8, fr_off(lane), 512) : L_AIMG + fr_off(lane);
      int po = isX ? sel(b16 < 10, b16 * 64 + g4 * 16, OOB) : (b16 >> 3) * P5F + p5_jb((lane & 7) >> 1) + (lane & 1) * 64 + g4 * 16;
      PIN(bo); PIN(po);
      lds_wait(F_T2, 4u * live);
      ST(8);
      v4u a[4][4];
#pragma unroll
      for (int c = 0; c < 4; ++c)
#pragma unroll
        for (int t = 0; t < 3; ++t) a[c][t] = trfrag(L, L_RA + tr_base(lane, 512, 4 * mw + c) + t * 32 * 512, 16 * 512);
      lds_wait(F_T2 + 1, 4u * live);
#pragma unroll
      for (int c = 0; c < 4; ++c) a[c][3] = trfrag(L, L_RA + tr_base(lane, 512, 4 * mw + c) + 3 * 32 * 512, 16 * 512);
      ST(9);
      barr_if(live);
      ST(10);
      v4u bf[4];
#pragma unroll
      for (int t = 0; t < 4; ++t) bf[t] = ldsr128(L, bo + t * (isX ? 640 : 512));
      __builtin_amdgcn_sched_barrier(0);
      v4f acc[4] = {};
#pragma unroll
      for (int t = 0; t < 4; ++t) { acc[0] = mfma(a[0][t], bf[t], acc[0]); acc[1] = mfma(a[1][t], bf[t], acc[1]); }
      PIN(acc[0]); PIN(acc[1]);
      __builtin_amdgcn_sched_barrier(0);
#pragma unroll
      for (int t = 0; t < 4; ++t) { acc[2] = mfma(a[2][t], bf[t], acc[2]); acc[3] = mfma(a[3][t], bf[t], acc[3]); }
      __builtin_amdgcn_sched_barrier(0);
      const v4u p0 = pk8(acc[0], acc[1]);
      if (isX) st128(p0, rs, po, ps3); else KEEP(p0);
      __builtin_amdgcn_sched_barrier(0);
      const v4u p1 = pk8(acc[2], acc[3]);
      if (isX) {
        st128(p1, rs, po, ps3 + P4F);
        KEEP(p0);
      } else {
        const bool lo = b16 < 8;
        const v4u o = {lo ? p0[0] : p1[0], lo ? p0[1] : p1[1], lo ? p0[2] : p1[2], lo ? p0[3] : p1[3]};
        st128(o, rs, po, ps3);
      }
      ST(11);
    }
  }
  STDUMP();
}

template <bool isX>
__device__ __forceinline__ void grole(const Params& p, LAS char* L, const int lane0, const int wave, const rsrc_t rs, const unsigned t_in) {
  const int xcd = bidx() & 7, ci = bidx() >> 4, cid = xcd * 16 + ci, id5 = xcd * 14 + ci;
  const int gw = wave - 8;
  const int gf = gw & 3, gbh = gw >> 2;
  const v4f z4 = {0.f, 0.f, 0.f, 0.f};
  v4u rw = mksrd(p.ws, WS_END);
  unsigned one = 1u;
  int fh = L_FLAG + F_H * 4;
  PINS(rw); PIN(one); PIN(fh);
  constexpr float SC = 0.08838834764831845f, L2E = 1.4426950408889634f;
  const u64 tag = get_tag(p.ws);
  unsigned nleft = SPIN_CAP, more;
  if (isX) {
    u64 outp;
    asm volatile("s_load_dwordx2 %0, %1, 0x40\n\ts_waitcnt lgkmcnt(0)" : "=s"(outp) : "s"(p.out));
    waitvm<0>();
    int flip;
    {
      __builtin_amdgcn_s_setprio(2);
      if (live || gw < 4) {
        FRESH(lane);
        const int b16 = lane & 15, g4 = lane >> 4;
        int qo = q_at(xcd, (ci >> 2) * 8 + gw), qo1 = qo + 1024;
        PINS(qo); PINS(qo1);
        const int qb = qo;
        const int d = 4 * (lane & 31) + 2 * (lane >> 5);
        int fo = L_QIMG + fr_off(lane), qi = L_QIMG + (d >> 3) * 128 + gw * 16 + (d & 7) * 2, mi = L_JUNK + live * (L_MX - L_JUNK) + (lane & 7) * 4, l16 = lane * 16;
        int pi = L_PIMG + gw * 512 + fr_off(lane) + (b16 >> 3) * 8;
        unsigned qt = qtag(tag, lane);
        int ko[4];
#pragma unroll
        for (int t = 0; t < 4; ++t) { ko[t] = L_RA + (32 * gw + b16) * 256 + (((4 * t + g4) ^ b16) << 4); PIN(ko[t]); }
        int mls = ml_at(ci >> 2, xcd) + (ci & 3) * 128, mlo = sel(lane < 8, lane * 8, OOB), lo = L_PIMG + (b16 >> 3) * 2048 + fr_off(lane);
        PINS(mls); PIN(mlo); PIN(lo);
        PIN(fo); PIN(qi); PIN(mi); PIN(l16); PIN(pi); PIN(qt);
        ST(0);
        lds_wait(F_INIT, 4u);
        v4u q0, q1, kf[2][4];

        do {
        if (QDUAL & 2) {
          const int fl = flip_wait();
          hint_wait<32, 32, 2, HD0>(p.ws + (fl ? WS_H02 + (cid ^ 64) * 128 : hint_at(0, cid)), gw, one, fh);
          qo = qb ^ fl; qo1 = qo + 1024;
        } else {
          hint_wait<32, 32, 2, HD0>(p.ws + hint_at(0, cid), gw, one, fh);
          if (QDUAL) { qo = qb ^ (__builtin_amdgcn_readfirstlane(lds_wait(F_P, 0u)) & 8192); qo1 = qo + 1024; }
        }
        STB(13);
          if (LIKELY(live)) { q0 = ld128(rs, l16, qo); q1 = ld128(rs, l16, qo1); } else { FAKE(q0); FAKE(q1); }
          lds_wait(F_K, 4u * live);
#pragma unroll
          for (int t = 0; t < 4; ++t) {
            kf[0][t] = ldsr128(L, ko[t]);
            kf[1][t] = ldsr128(L, ko[t] + 4096);
          }
          __builtin_amdgcn_sched_barrier(0);
          const v4f qs = __builtin_bit_cast(v4f, q0 & 0xFFFFFF00u) + __builtin_bit_cast(v4f, q1 & 0xFFFFFF00u);
          *(LAS unsigned*)(L + qi) = pk(rs32(qs[0], qs[2]), rs32(qs[1], qs[3]));
          const unsigned t0 = __builtin_amdgcn_perm(__builtin_amdgcn_perm(q0[3], q0[2], 0x0c0c0400u), __builtin_amdgcn_perm(q0[1], q0[0], 0x0c0c0400u), 0x05040100u);
          const unsigned t1 = __builtin_amdgcn_perm(__builtin_amdgcn_perm(q1[3], q1[2], 0x0c0c0400u), __builtin_amdgcn_perm(q1[1], q1[0], 0x0c0c0400u), 0x05040100u);
          more = again_q(nleft, (t0 ^ qt) | (t1 ^ qt));
        } while (UNLIKELY(more));
        STB(7);
        bar_if(live);
        STB(4); STN(3);
        v4u qf[4];
#pragma unroll
        for (int t = 0; t < 4; ++t) qf[t] = ldsr128(L, fo + t * 512);
        __builtin_amdgcn_sched_barrier(0);
        v4f sc[2] = {};
#pragma unroll
        for (int t = 0; t < 4; ++t) {
          sc[0] = mfma(kf[0][t], qf[t], sc[0]);
          sc[1] = mfma(kf[1][t], qf[t], sc[1]);
        }
        const v4f sr = sel4(b16 < 8, sc[0], sc[1]);
        asm volatile("ds_max_f32 %0, %1" ::"v"(mi), "v"(max3(max3(sr[0], sr[1], sr[2]), sr[3], sr[3])) : "memory");
        float s[4];
#pragma unroll
        for (int i = 0; i < 2; ++i) {
          const unsigned r1 = pk(sr[2 * i], sr[2 * i + 1]);
          const unsigned r2 = pk(bflo(r1) * SC, bfhi(r1) * SC);
          s[2 * i] = bflo(r2); s[2 * i + 1] = bfhi(r2);
        }
        KEEP(s[0]); KEEP(s[1]); KEEP(s[2]); KEEP(s[3]);
        bar_if(live);
        STB(1);
        const unsigned fw = *(LAS unsigned*)(L + L_FLAG + F_P * 4);
        const float M = rbf(rbf(*(LAS float*)(L + mi)) * SC);
#pragma unroll
        for (int i = 0; i < 4; ++i) s[i] = __builtin_amdgcn_exp2f((s[i] - M) * L2E);
        *(LAS v2u*)(L + pi) = v2u{pk(s[0], s[1]), pk(s[2], s[3])};
        bar_if(live);
        STB(6);
        flip = __builtin_amdgcn_readfirstlane(fw) & 8192;
        int gwo = gw;
        if (LIKELY(gw == 0)) {
          float l = 0.f, l2 = 0.f;
#pragma unroll
          for (int t = 0; t < 4; ++t) {
            const v4u pf = ldsr128(L, lo + t * 512);
            l = dot2(pf[0], 0x3F803F80u, l); l2 = dot2(pf[1], 0x3F803F80u, l2); l = dot2(pf[2], 0x3F803F80u, l); l2 = dot2(pf[3], 0x3F803F80u, l2);
          }
          l += l2;
          l += dpp<ROR8>(l);
          l = rs16(l, l); l = rs32(l, l);
          const v2u ml = {__float_as_uint(M * L2E), __float_as_uint(l)};
          if (LIKELY(live)) st64(ml, rs, mlo, mls ^ flip); else KEEP(ml);
        }
        asm volatile("" : "+s"(gwo));
        if (gwo == 3) { if (S_XC) __builtin_amdgcn_s_sleep(S_XC);
          const int xm = (flip && YM != 1) ? xcd ^ 4 : xcd;
          const unsigned i5 = ((lane - 16) & 31) * 4 + (xm >> 1);
          rdy_check(sel(lane < 16, 128 + lane * 8 + xm, sel(lane < 44, 128 + i5 + (i5 / 14) * 2, (lane & 7) * 16 + (flip ? pmir(ci) : ci))), rs, tag);
        }
      }
      if (live || gw >= 4) {
        FRESH(lane);
        const int b16 = lane & 15, g4 = lane >> 4;
        v4u asum = sel_sum(lane);
        {
          const int gb = 4 * gbh + ((lane >> 2) & 3);
          const int gk = 16 * (lane >> 5) + 4 * (lane & 3) + 2 * (g4 & 1);
          const unsigned xpair = *(LAS unsigned*)(L + L_X + gb * 256 + (gf * 32 + gk) * 2);
          float x0 = bflo(xpair), x1 = bfhi(xpair);
          int ps = p3_at(xcd, gf) ^ flip, ps1 = ps + 2048, ps2 = ps + 4096, ps3 = ps + 6144, sts = (st_at(ci) + xcd * 128) ^ flip;
          unsigned two = 2u;
          PINS(ps); PINS(ps1); PINS(ps2); PINS(ps3); PINS(sts); PIN(two);
          int po = g4 * 512 + gb * 64 + (lane & 3) * 16, hi = L_HIMG + gf * 640 + (lane & 3) * 128 + gb * 16 + g4 * 4;
          const unsigned i3 = ci * 8 + lane;
          int h3 = sel(lane < (ci < 14 ? 8 : 0), hint_at(3, i3 + (i3 / 14) * 2) ^ flip, OOB);
          int ho = L_HIMG + sel(b16 < 8, fr_off(lane), 512);
          int sa = sel((b16 < 8) & ((g4 == 2) | (g4 == (b16 >> 2))), b16 * 8 + sel(g4 == 2, 0, 4), OOB);
          PIN(asum); PIN(x0); PIN(x1); PIN(po); PIN(hi); PIN(h3); PIN(ho); PIN(sa);
          lds_wait(F_RDY, live);
          v4u v[4];

          do {
          hint_wait<16, 64, 6, HD2>(p.ws + (hint_at(2, cid) ^ flip), gw, two, fh);
          STB(14);
            if (LIKELY(live)) {
              v[0] = ld128(rs, po, ps); v[1] = ld128(rs, po, ps1); v[2] = ld128(rs, po, ps2); v[3] = ld128(rs, po, ps3);
            } else { FAKE(v[0]); FAKE(v[1]); FAKE(v[2]); FAKE(v[3]); }
            if (ci < 14) HINT_PRE(3, F_T2 + 1);
            const v4f r = mfma(asum, v[3], mfma(asum, v[2], mfma(asum, v[1], mfma(asum, v[0], z4))));
            const unsigned at = pk(r[0], r[1]);
            *(LAS unsigned*)(L + hi) = pk(x0 + bflo(at), x1 + bfhi(at));
            more = again(nleft, r[0], r[1], v[0], v[1], v[2], v[3]);
          } while (UNLIKELY(more));
          HINT_BAR(3, h3);
          STB(9); STN(8);
          if (LIKELY(gw == 4)) {
            v4f gm = {};
#pragma unroll
            for (int t = 0; t < 4; ++t) v[t] = ldsr128(L, ho + t * 640);
            __builtin_amdgcn_sched_barrier(0);
#pragma unroll
            for (int t = 0; t < 4; ++t) gm = mfma(v[t], v[t], gm);
            const float gv = self(lane & 2, self(lane & 1, gm[3], gm[2]), self(lane & 1, gm[1], gm[0]));
            if (LIKELY(live)) st32(__float_as_uint(gv), rs, sa, sts); else KEEP(gv);
          }
        }
        int gwo = gw;
        asm volatile("" : "+s"(gwo));
        if (gwo == 5) {
          const int f = ci >> 2, jb = ci & 3;
          const int ob = 2 * jb + ((lane >> 2) & 1), og = lane & 3, oj = 2 * g4 + ((lane >> 3) & 1);
          float hv = bflo(*(LAS u16*)(L + L_HIMG + f * 640 + og * 128 + ob * 16 + oj * 2));
          int ps = (p5_at(xcd * 4 + f) + p5_jb(jb)) ^ flip, ps1 = ps + 1024, ps2 = ps + 2048;
          PINS(ps); PINS(ps1); PINS(ps2);
          int oo = (ob * 1024 + xcd * 128 + f * 32 + 16 * (oj >> 2) + 4 * og + (oj & 3)) * 2, l16 = lane * 16, l3 = sel(lane < 32, lane * 16 + 3072, OOB);
          v2u sq;
          if (live) {
            __builtin_amdgcn_s_sleep(ST_SLEEP);
#pragma unroll 1
            for (unsigned n = 0; n < SPIN_CAP; ++n) {
              sq = ld64(rs, (lane >> 3) * 128 + ob * 8, st_at(ci) ^ flip);
              if (__builtin_amdgcn_ballot_w64((sq[0] == SENT) | (sq[1] == SENT)) == 0) break;
              __builtin_amdgcn_s_sleep(4);
            }
          } else FAKE(sq);
          float s = __uint_as_float(sq[0]), q = __uint_as_float(sq[1]);
          s += dpp<ROR8>(s); q += dpp<ROR8>(q);
          s = rs16(s, s); q = rs16(q, q); s = rs32(s, s); q = rs32(q, q);
          float inv = ln_inv(s * (1.f / 1024.f), q * (1.f / 1024.f));
          PIN(hv); PIN(oo); PIN(l16); PIN(l3); PIN(inv);
          v4u v[4]; v4f r; unsigned res;

          do {
          hint_poll<28, 64>(p.ws, live, 4, cid ^ (flip >> 7), 0u);
          if (HD4) __builtin_amdgcn_s_sleep(HD4);
          ST(15);
            if (LIKELY(live)) {
              v[0] = ld128(rs, l16, ps); v[1] = ld128(rs, l16, ps1); v[2] = ld128(rs, l16, ps2); v[3] = ld128(rs, l3, ps);
            } else { FAKE(v[0]); FAKE(v[1]); FAKE(v[2]); FAKE(v[3]); }
            r = mfma(asum, v[3], mfma(asum, v[2], mfma(asum, v[1], mfma(asum, v[0], z4))));
            const float u0 = r[0] + dpp<ROR8>(r[0]), u1 = r[1] + dpp<ROR8>(r[1]);
            res = pk(hv + rbf(self(lane & 8, u1, u0) * inv), 0.f);
            KEEP(res);
            more = again(nleft, u0, u1, v[0], v[1], v[2], v[3]);
          } while (UNLIKELY(more));
          if (LIKELY(live)) asm volatile("global_store_short %0, %1, %2 sc1" ::"v"(oo), "v"(res), "s"(outp) : "memory"); else KEEP(res);
          STB(12); STN(11);
          if (cid == 0 && live) st64(v2u{(unsigned)tag, (unsigned)(tag >> 32)}, rs, sel(lane == 0, 0, OOB), WS_E);
        }
      }
    }
  } else {
    {
      __builtin_amdgcn_s_setprio(2);
      if (gw == 5 && live) {
        const unsigned tb = now();
        wait_until(tb + RDY_A);
        const int fl = flip_read(0, 0, rs, tag);
        const unsigned j5 = ci < 14 ? id5 ^ 2 : 0;
        const int pn = ci < 14 ? id5 >> 3 : 0;
        const int wx = sel(lane0 < 32, (lane0 >> 2) * 16 + ((cid >> 5) ^ (fl >> 12)) * 4 + (lane0 & 3), (lane0 & 7) * 16 + (fl ? pmir(pn) : pn));
        const int wy = 128 + sel(lane0 & 1, cid ^ 4, j5 + (j5 / 14) * 2);
        if (YM) { wait_until(tb + RDY_C); rdy_check(wx, rs, tag); }
        else if (RDY_B) {
          rdy_check(wx, rs, tag, L_JUNK);
          wait_until(tb + RDY_B);
          rdy_check(wy, rs, tag);
        } else rdy_check(sel(lane0 < 48, wx, wy), rs, tag);
      }
      const int flip = flip_wait();
      {
        FRESH(lane);
        const int b16 = lane & 15, g4 = lane >> 4;
        v4u as0 = sel_split(lane, 0), as1 = sel_split(lane, 1);
        const int rs3 = cid >> 3;
        int os = o_at(rs3, gw) ^ flip, ms = ml_at(rs3 >> 2, gw) ^ flip, os1 = os + 256, ms1 = ms + 128;
        PINS(os); PINS(ms); PINS(os1); PINS(ms1);
        int oo = g4 * 512 + b16 * 16, mo = (lane >> 5) * 256 + (4 * (g4 & 1) + (b16 >> 2)) * 8;
        int h2 = sel(lane < 16, hint_at(2, (cid & 7) * 16 + lane) ^ flip, OOB), oi = L_OIMG + (b16 >> 2) * 512 + (lane & 3) * 128 + gw * 16 + g4 * 4;
        PIN(as0); PIN(as1); PIN(oo); PIN(mo); PIN(h2); PIN(oi);
        lds_wait(F_RDY, live);
        lds_wait(F_INIT, 4u);
        v4u v0, v1; v2u e0, e1;

        do {
        hint_wait<32, 64, 6, HD1>(p.ws + (hint_at(1, cid) ^ flip), gw, one, fh);
        STB(13);
          if (LIKELY(live)) {
            e0 = ld64(rs, mo, ms); e1 = ld64(rs, mo, ms1);
            __builtin_amdgcn_sched_barrier(0);
            v0 = ld128(rs, oo, os); v1 = ld128(rs, oo, os1);
          } else { FAKE(e0); FAKE(e1); FAKE(v0); FAKE(v1); }
          HINT_PRE(2, F_T1 + 1);
          const auto ma = __builtin_amdgcn_permlane32_swap(e0[0], e0[0], false, false), mb = __builtin_amdgcn_permlane32_swap(e1[0], e1[0], false, false);
          const float m0 = __uint_as_float(ma[0]), m1 = __uint_as_float(mb[0]), m2 = __uint_as_float(ma[1]), m3 = __uint_as_float(mb[1]);
          const float Mx = max3(max3(m0, m1, m2), m3, m3);
          const float f0 = __builtin_amdgcn_exp2f(__uint_as_float(e0[0]) - Mx), f1 = __builtin_amdgcn_exp2f(__uint_as_float(e1[0]) - Mx);
          const float lp = f0 * __uint_as_float(e0[1]) + f1 * __uint_as_float(e1[1]);
          const float inv = __builtin_amdgcn_rcpf(rs32(lp, lp));
          const unsigned w0 = __float_as_uint(f0 * inv), w1 = __float_as_uint(f1 * inv);
          const auto h0 = __builtin_amdgcn_permlane16_swap(w0, w0, false, false), h1 = __builtin_amdgcn_permlane16_swap(w1, w1, false, false);
          const auto a0 = __builtin_amdgcn_permlane32_swap(h0[0], h0[0], false, false), a1 = __builtin_amdgcn_permlane32_swap(h1[0], h1[0], false, false);
          const auto b0 = __builtin_amdgcn_permlane32_swap(h0[1], h0[1], false, false), b1 = __builtin_amdgcn_permlane32_swap(h1[1], h1[1], false, false);
          const v4f d00 = mfma(as0, v0, z4), d01 = mfma(as1, v0, z4), d10 = mfma(as0, v1, z4), d11 = mfma(as1, v1, z4);
          const v2f oa = (d00.lo * __uint_as_float(a0[0]) + d00.hi * __uint_as_float(a1[0])) + (d01.lo * __uint_as_float(a0[1]) + d01.hi * __uint_as_float(a1[1]));
          const v2f ob = (d10.lo * __uint_as_float(b0[0]) + d10.hi * __uint_as_float(b1[0])) + (d11.lo * __uint_as_float(b0[1]) + d11.hi * __uint_as_float(b1[1]));
          *(LAS unsigned*)(L + oi) = pk(oa[0], oa[1]);
          *(LAS unsigned*)(L + oi + 2048) = pk(ob[0], ob[1]);
          more = again(nleft, oa[0], ob[0], v0, v1, v4u{e0[0], e0[1], e1[0], e1[1]}, v1);
        } while (UNLIKELY(more));
        HINT_BAR(2, h2);
        STB(3); STN(2);
      }
      if (ci < 14) {
        FRESH(lane);
        const int g4 = lane >> 4;
        const int gb = 4 * gbh + ((lane >> 2) & 3);
        v4u asum = sel_sum(lane);
        int ps = p4_at((id5 >> 2) * 4 + gf) ^ flip, ss = st_at(id5 >> 3) ^ flip, ps1 = ps + 4 * 640;
        unsigned two = 2u;
        PINS(ps); PINS(ss); PINS(ps1); PIN(two);
        int po = g4 * 640 + gb * 64 + (lane & 3) * 16, co = (g4 + 4 * ((lane >> 3) & 1)) * 640 + 512 + (lane & 3) * 16;
        int so = ((lane & 3) + 4 * (g4 & 1)) * 128 + gb * 8;
        int h4 = sel(lane < 32, hint_at(4, (id5 & 3) * 32 + lane) ^ flip, OOB), ai = L_AIMG + gf * 512 + (lane & 3) * 128 + gb * 16 + g4 * 4;
        PIN(asum); PIN(po); PIN(co); PIN(so); PIN(h4); PIN(ai);
        v4u v0, v1, cv; unsigned sq;

        do {
        hint_wait<8, 64, 6, HD3>(p.ws + (hint_at(3, cid) ^ flip), gw, two, fh);
        STB(14);
          if (LIKELY(live)) {
            sq = ld32(rs, so, ss); cv = ld128(rs, co, ps);
            v0 = ld128(rs, po, ps); v1 = ld128(rs, po, ps1);
          } else { FAKE(sq); FAKE(cv); FAKE(v0); FAKE(v1); }
          HINT_PRE(4, F_T2 + 1);
          float s = __uint_as_float(sq);
          s += dpp<XOR1>(s); s += dpp<XOR2>(s);
          const float m = rbf(rs16(s, s) * (1.f / 1024.f));
          const v4f rc = mfma(asum, cv, z4);
          const float ca = rc[0] + dpp<ROR8>(rc[0]), cb = rc[1] + dpp<ROR8>(rc[1]);
          const v4f r = mfma(asum, v1, mfma(asum, v0, z4));
          const float u0 = r[0] - m * ca, u1 = r[1] - m * cb;
          *(LAS unsigned*)(L + ai) = relu2(pk(u0, u1));
          KEEP(rc); KEEP(r);
          more = again(nleft, u0, u1, v0, v1, cv, v4u{sq, sq, sq, sq});
        } while (UNLIKELY(more));
        HINT_BAR(4, h4);
        STB(5); STN(4);
      }
    }
  }
  STDUMP();
}

__global__ void __launch_bounds__(NTHR, 4) fused(Params) {
  asm volatile(".subsection 2\n\t.fill 128, 4, 0xBF800000\n\t.subsection 0");
  typedef unsigned v16u __attribute__((ext_vector_type(16)));
  v16u ka;
  u64 t64;
  const auto kap = __builtin_amdgcn_kernarg_segment_ptr();
  asm volatile("s_load_dwordx16 %0, %2, 0x0\n\ts_memrealtime %1" : "=&s"(ka), "=&s"(t64) : "s"(kap));
  __builtin_amdgcn_sched_barrier(0);
  extern __shared__ __attribute__((aligned(16))) char smem[];
#if defined(__HIP_DEVICE_COMPILE__)
  LAS char* L = __builtin_bit_cast(LAS char*, 0u);
  asm volatile("" ::"s"((LAS char*)smem));
#else
  LAS char* L = (LAS char*)smem;
#endif
  const int lane0 = threadIdx.x & 63, wave = __builtin_amdgcn_readfirstlane(threadIdx.x >> 6);
  const int cid = (bidx() & 7) * 16 + (bidx() >> 4);
  int kind = (wave >> 2) + ((bidx() & 8) ? 0 : 4);
#define QSTEP(o) (64 * 8192)
  if (kind == 0) {
    int vo = vo_w256(lane0, wave, 8192), so = (cid & 3) * 16 * 8192 + (cid >> 2) * 256, dst = L_RA + wave * 1024;
    asm volatile("s_waitcnt lgkmcnt(0)" : "+s"(ka), "+s"(t64), "+s"(so), "+v"(vo), "+s"(dst));
    const v4u srd = {ka[0], ka[1] & 0xFFFFu, 1024u * 8192u, 0x00020000u};
#pragma unroll 1
    for (int o = 0; o < N_Y0; ++o) { dma1(srd, dst, vo, so); so += QSTEP(o); dst += 4096; }
    waitvm<N_Y0 - 1>();
    if (TREF == 2) t64 = __builtin_amdgcn_s_memrealtime() - TREF2_OFF;
#pragma unroll 1
    for (int o = N_Y0; o < 16; ++o) { waitvm<W_F - 1>(); dma1(srd, dst, vo, so); so += QSTEP(o); dst += 4096; }
    STX(1);
    waitvm<4>();
  }
  asm volatile("" : "+s"(kind));
  if (XF && kind == 4) {
    int vo = vo_k(lane0, wave), so = kv_base(), dst = L_RA + wave * 1024;
    asm volatile("s_waitcnt lgkmcnt(0)" : "+s"(ka), "+s"(t64), "+s"(so), "+v"(vo), "+s"(dst));
    const v4u srd = {ka[6], ka[7] & 0xFFFFu, 8u * 4096u * 1024u, 0x00020000u};
    stream<64, 0, true>(0, N_K0, srd, dst, vo, so, 8192, 4096);
    if (XF == 2) { asm volatile("s_barrier" ::: "memory"); stream<W_KA, P_KA, true>(N_K0, N_KA, srd, dst, vo, so, 8192, 4096); }
  }
  asm volatile("" : "+s"(kind));
  if (kind == 5) {
    const u64 pc = __builtin_amdgcn_s_getpc();
    const unsigned co = (((lane0 & 3) * 4 + wave - 4) * 16 + (bidx() >> 4)) * 128;
    asm volatile("s_mov_b32 m0, %2\n\ts_nop 0\n\tglobal_load_lds_dword %0, %1" ::"v"(co < CODE_BYTES ? co : 0u), "s"(pc), "s"(L_JUNK) : "memory");
  }
  asm volatile("s_waitcnt lgkmcnt(0)" : "+s"(ka), "+s"(t64), "+s"(kind));
  Params p;
#define KP(i) (((u64)ka[2 * (i) + 1] << 32) | ka[2 * (i)])
  p.wq = (const u16*)KP(0); p.x = (const u16*)KP(1); p.ws = (char*)KP(2); p.kc = (const u16*)KP(3);
  p.vc = (const u16*)KP(4); p.wo = (const u16*)KP(5); p.w1 = (const u16*)KP(6); p.w2 = (const u16*)KP(7);
  p.out = (u16*)(u64)kap;
  if ((kind | 1) == 3) {
    const int gw = wave - 8, lane = lane0;
    asm volatile("s_setprio 3\n\ts_mov_b32 m0, %2\n\ts_nop 0\n\tglobal_load_lds_dwordx4 %0, %1\n\tglobal_load_lds_dwordx4 %0, %1 offset:1024"
                 ::"v"(lane * 16), "s"((u64)p.x + gw * 2048), "s"(L_X + gw * 2048) : "memory");
    {
      ST(7);
      if (live) waitvm<0>();
      ST(1);
      const v4u x0 = ldsr128(L, L_X + gw * 2048 + lane * 16), x1 = ldsr128(L, L_X + gw * 2048 + 1024 + lane * 16);
      const v2u xq = *(LAS v2u*)(L + L_X + gw * 2048 + (lane >> 2) * 128 + (cid & 3) * 32 + (lane & 3) * 8);
      float s = 0.f, q = 0.f, s2 = 0.f, q2 = 0.f;
#pragma unroll
      for (int i = 0; i < 4; ++i) {
        s = dot2(x0[i], 0x3F803F80u, s); q = dot2(x0[i], x0[i], q);
        s2 = dot2(x1[i], 0x3F803F80u, s2); q2 = dot2(x1[i], x1[i], q2);
      }
      s = rs32(s + s2, q + q2);
      s += dpp<XOR1>(s); s += dpp<XOR2>(s); s += dpp<ROR4>(s); s += dpp<ROR8>(s);
      s = rs16(s, s) * (1.f / 1024.f);
      const float mean = __builtin_bit_cast(float, __builtin_amdgcn_readlane(__builtin_bit_cast(int, s), 0));
      const float inv = ln_inv(mean, __builtin_bit_cast(float, __builtin_amdgcn_readlane(__builtin_bit_cast(int, s), 32))), m = rbf(mean);
      const unsigned c0 = pk(bflo(xq[0]) - m, bfhi(xq[0]) - m), c1 = pk(bflo(xq[1]) - m, bfhi(xq[1]) - m);
      const v2u y = {pk(bflo(c0) * inv, bfhi(c0) * inv), pk(bflo(c1) * inv, bfhi(c1) * inv)};
      *(LAS v2u*)(L + L_YIMG + (lane >> 3) * 512 + (lane & 3) * 128 + gw * 16 + ((lane >> 2) & 1) * 8) = y;
      ST(0);
    }
  }
  const rsrc_t rs = mkrs(p.ws, WS_END);
  if (wave == 5)
    st128(v4u{SENT, SENT, SENT, SENT}, rs, sel((lane0 >> 3) < 3 - (kind == 1), (2 * (lane0 >> 3) + (kind == 1)) * 16384 + (lane0 & 7) * 16, OOB), hint_at(0, cid));
  if ((QDUAL & 2) && kind == 5 && wave == 6) st128(v4u{SENT, SENT, SENT, SENT}, rs, sel(lane0 < 8, lane0 * 16, OOB), WS_H02 + (cid ^ 64) * 128);
  if (wave == 4) lds_set(L_FLAG + (lane0 & 31) * 4, (lane0 & 16) ? 0xFF800000u : 0u);
  asm volatile("" : "+s"(kind));
  if (kind == 1) {
    const int lane = lane0, mw = wave - 4, b16 = lane & 15;
    const int tb0 = L_RA + tr_base(lane, 256, 2 * mw), tb1 = L_RA + tr_base(lane, 256, 2 * mw + 1), fo = L_YIMG + fr_off(lane);
    const int qo = q_at(lane & 7, cid >> 2) + (b16 >> 3) * 64 + (lane >> 4) * 16, qs = (cid & 3) * 512 + mw * 128;
    unsigned qt = 0;
    int qo2 = qo ^ 8192;
    PIN(qo2);
    {
      __builtin_amdgcn_s_setprio(3);
      if (live) qt = qtag(get_tag(p.ws), lane >> 4);
      ST(14);
      bar_if(live);
      ST(0);
      v4u a[2][8], bf[8];
      v4f acc[2] = {};
#pragma unroll
      for (int t = 0; t < 6; ++t) { bf[t] = ldsr128(L, fo + t * 512); a[0][t] = trfrag(L, tb0 + t * 32 * 256, 16 * 256); a[1][t] = trfrag(L, tb1 + t * 32 * 256, 16 * 256); }
      bf[6] = ldsr128(L, fo + 6 * 512); bf[7] = ldsr128(L, fo + 7 * 512);
      __builtin_amdgcn_sched_barrier(0);
#pragma unroll
      for (int t = 0; t < S1_AT; ++t) { acc[0] = mfma(a[0][t], bf[t], acc[0]); acc[1] = mfma(a[1][t], bf[t], acc[1]); }
      PIN(acc[0]); PIN(acc[1]);
      lds_wait(F_Q, 4u * live);
      ST(2);
#pragma unroll
      for (int t = 6; t < 8; ++t) { a[0][t] = trfrag(L, tb0 + t * 32 * 256, 16 * 256); a[1][t] = trfrag(L, tb1 + t * 32 * 256, 16 * 256); }
      __builtin_amdgcn_sched_barrier(0);
#pragma unroll
      for (int t = S1_AT; t < 8; ++t) { acc[0] = mfma(a[0][t], bf[t], acc[0]); acc[1] = mfma(a[1][t], bf[t], acc[1]); }
      __builtin_amdgcn_sched_barrier(0);
      const v4u w = __builtin_bit_cast(v4u, sel4(b16 < 8, acc[0], acc[1]));
      v4u o;
#pragma unroll
      for (int k = 0; k < 4; ++k) o[k] = __builtin_amdgcn_perm(w[k] + 0x80u, qt, 0x07060500u + k);
      st128(o, rs, qo, qs);
      if (QDUAL) st128(o, rs, qo2, qs);
      ST(3);
    }
    lds_inc(inc_addr(lane, F_QR));
  } else {
    if (!XF && kind == 4) {
      stream<64, 0, true>(0, N_K0, mksrd(p.kc, 8 * 4 * 4096 * 256), L_RA + wave * 1024, vo_k(lane0, wave), kv_base(), 8192, 4096);
      STX(1);
    }
    if (kind >= 6)
      asm volatile("s_mov_b32 m0, %2\n\ts_nop 0\n\tglobal_load_lds_dword %0, %1"
                   ::"v"(lane0 * 4), "s"(p.x + (wave - 8) * 1024 + (bidx() & 7) * 128), "s"(L_X + (wave - 8) * 256) : "memory");
    const int fa = inc_addr(lane0, F_Q);
    const int c0 = ((lane0 >> 2) & 7) * 16 + (cid >> 5) * 4 + (lane0 & 3);
    const int h0 = sel(lane0 < 32, hint_at(0, c0), (QDUAL & 2) ? WS_H02 + (c0 ^ 64) * 128 : OOB);
    if (XF != 2 || kind != 4) bar();
    if (kind == 7 && wave == 15 && cid == 16) flip_decide(p.ws, rs, lane0);
    if (kind == 0) { waitvm<0>(); lds_inc(fa); }
    if (kind == 2 && wave == 8) {
      if (((cid >> 2) & 3) == 1) lds_wait(F_Q, 4u);
      else __builtin_amdgcn_s_sleep(H0);
      hint_send(mksrd(p.ws, WS_END), h0);
    }
  }
  const unsigned t_in = (unsigned)t64;
  asm volatile("" : "+s"(kind));
  if (kind == 0) { loader<false>(p, L, lane0, wave, t_in); __builtin_amdgcn_endpgm(); }
  if (kind == 4) { STV(0, t_in); loader<true>(p, L, lane0, wave, t_in); __builtin_amdgcn_endpgm(); }
  if (kind == 1) { mrole<false>(p, L, lane0, wave, rs, t_in); __builtin_amdgcn_endpgm(); }
  if (kind == 5) { mrole<true>(p, L, lane0, wave, rs, t_in); __builtin_amdgcn_endpgm(); }
  if (kind >= 6) { grole<true>(p, L, lane0, wave, rs, t_in); __builtin_amdgcn_endpgm(); }
  grole<false>(p, L, lane0, wave, rs, t_in);
}
}

extern "C" void kernel_launch(void* const* d_in, const int* in_sizes, int n_in, void* d_out, int out_size, void* d_ws, size_t ws_size,
                              hipStream_t stream) {
  mk::Params p;
  p.x = (const mk::u16*)d_in[0]; p.kc = (const mk::u16*)d_in[1]; p.vc = (const mk::u16*)d_in[2];
  p.wq = (const mk::u16*)d_in[3]; p.wo = (const mk::u16*)d_in[4]; p.w1 = (const mk::u16*)d_in[6]; p.w2 = (const mk::u16*)d_in[7];
  p.out = (mk::u16*)d_out; p.ws = (char*)d_ws + WS_OFF(ws_size);
  (void)hipFuncSetAttribute((const void*)mk::fused, hipFuncAttributeMaxDynamicSharedMemorySize, mk::L_END);
  mk::fused<<<256, mk::NTHR, mk::L_END, stream>>>(p);
}
```
